```python
import torch

def _softmax(x, axis=-1):
    return torch.softmax(x, dim=axis)
def _gelu(x):
    return torch.nn.functional.gelu(x, approximate="tanh")
def _silu(x):
    return torch.nn.functional.silu(x)
def _sigmoid(x):
    return torch.sigmoid(x)
def _layernorm(x, axis=-1, eps=1e-5):
    m = x.mean(dim=axis, keepdim=True)
    v = x.var(dim=axis, keepdim=True, unbiased=False)
    return (x - m) / torch.sqrt(v + eps)
def _scatter_add(tgt, idx, src, axis=0):
    return tgt.clone().index_add_(axis, idx, src)
def _segment_sum(src, seg, n):
    out = torch.zeros((n,) + src.shape[1:], dtype=src.dtype)
    return out.index_add_(0, seg, src)
def _pad(x, pw):
    flat = tuple(v for pair in reversed(pw) for v in pair)
    return torch.nn.functional.pad(x, flat)

kernel_name = 'parallel_sum__mha_block_cached__dense_activation_squared_relu__decode'

def setup_inputs():

    _w = (lambda a: (lambda t: t.to(torch.bfloat16) if t.is_floating_point() else t)(torch.as_tensor(a)))
    def _A_setup():
        B, Sq, D = 1, 1, 512
        H, Sk, Dh = 32, 65536, 128
        return {
            "x": _w((lambda *s: torch.randn(*s, dtype=torch.bfloat16) * 0.05)(B, Sq, D)),
            "k_cache": _w((lambda *s: torch.randn(*s, dtype=torch.bfloat16) * 3.2)(B, H, Sk, Dh)),
            "v_cache": _w((lambda *s: torch.randn(*s, dtype=torch.bfloat16) * 1.6)(B, H, Sk, Dh)),
            "wq": _w((lambda *s: torch.randn(*s, dtype=torch.bfloat16) * 3.2)(D, H * Dh) * (D ** -0.5)),
            "wo": _w((lambda *s: torch.randn(*s, dtype=torch.bfloat16) * 1.6)(H * Dh, D) * ((H * Dh) ** -0.5)),
        }
    _A_in = _A_setup()

    def _B_setup():
        B, S, D, F = 1, 1, 512, 1792
        return {
            "x": _w((lambda *s: torch.randn(*s, dtype=torch.bfloat16) * 0.05)(B, S, D)),
            "w1": _w((lambda *s: torch.randn(*s, dtype=torch.bfloat16) * 0.05)(D, F)),
            "b1": _w((lambda *s: torch.randn(*s, dtype=torch.bfloat16) * 0.05)(F)),
            "w2": _w((lambda *s: torch.randn(*s, dtype=torch.bfloat16) * 0.05)(F, D)),
            "b2": _w((lambda *s: torch.randn(*s, dtype=torch.bfloat16) * 0.05)(D)),
        }
    _B_in = _B_setup()

    return {
        "A_x": _A_in["x"],
        "A_k_cache": _A_in["k_cache"],
        "A_v_cache": _A_in["v_cache"],
        "A_wq": _A_in["wq"],
        "A_wo": _A_in["wo"],
        "B_x": _B_in["x"],
        "B_w1": _B_in["w1"],
        "B_b1": _B_in["b1"],
        "B_w2": _B_in["w2"],
        "B_b2": _B_in["b2"],
    }

def reference(A_x, A_k_cache, A_v_cache, A_wq, A_wo, B_x, B_w1, B_b1, B_w2, B_b2):
    def _A_ref(x, k_cache, v_cache, wq, wo):
        B, Sq, D = x.shape
        H, Dh = 32, 128
        q = (lambda x, *d: x.permute(*d))((x @ wq).reshape(B, Sq, H, Dh), 0, 2, 1, 3)
        scores = torch.einsum("bhqd,bhkd->bhqk", q, k_cache) * (Dh ** -0.5)
        p = _softmax(scores, axis=-1)
        o = torch.einsum("bhqk,bhkd->bhqd", p, v_cache)
        return (lambda x, *d: x.permute(*d))(o, 0, 2, 1, 3).reshape(B, Sq, H * Dh) @ wo

    def _B_ref(x, w1, b1, w2, b2):
        h = torch.einsum("bsd,df->bsf", x, w1) + b1
        zero = torch.zeros_like(h)
        r = torch.where(h > zero, h, zero)
        h = r * r
        out = torch.einsum("bsf,fd->bsd", h, w2) + b2
        return out

    x = A_x
    xn = _layernorm(x, axis=-1)
    return x + _A_ref(xn, A_k_cache, A_v_cache, A_wq, A_wo) + _B_ref(xn, B_w1, B_b1, B_w2, B_b2)
```

```cpp
#include <hip/hip_runtime.h>
#include <hip/hip_bf16.h>
#include <stdint.h>

#define EMUL 1

typedef unsigned int u32;
typedef u32 u32x4 __attribute__((ext_vector_type(4)));
typedef float f32x4 __attribute__((ext_vector_type(4)));
typedef __bf16 bf16x2 __attribute__((ext_vector_type(2)));

constexpr int D = 512, H = 32, SK = 65536, DH = 128, F = 1792;
constexpr int NSPLIT = 16;
constexpr int KEYS_PER_SPLIT = SK / NSPLIT;

__device__ __forceinline__ float bf2f(unsigned short b) { return __uint_as_float(((u32)b) << 16); }
__device__ __forceinline__ float rbf(float x) {
  __bf16 b = (__bf16)x; return (float)b;
}
__device__ __forceinline__ float dot2(u32 k, u32 q, float acc) {
  return __builtin_amdgcn_fdot2_f32_bf16(__builtin_bit_cast(bf16x2, k), __builtin_bit_cast(bf16x2, q), acc, false);
}
template <int CTRL> __device__ __forceinline__ float dpp(float x) {
  return __builtin_bit_cast(float, __builtin_amdgcn_mov_dpp(__builtin_bit_cast(int, x), CTRL, 0xf, 0xf, true));
}
constexpr int XOR1 = 0xB1, XOR2 = 0x4E, XOR7 = 0x141, XOR8 = 0x128;
__device__ __forceinline__ float row16_sum(float x) {
  x += dpp<XOR8>(x); x += dpp<XOR7>(x); x += dpp<XOR2>(x); x += dpp<XOR1>(x); return x;
}
__device__ __forceinline__ float wave_sum(float x) {
  for (int o = 32; o > 0; o >>= 1) x += __shfl_xor(x, o, 64);
  return x;
}

__device__ __forceinline__ void layernorm(const unsigned short* x, float* xn, float* red) {
  const int tid = threadIdx.x;
  float a = bf2f(x[tid]), b = bf2f(x[tid + 256]);
  float s = wave_sum(a + b);
  if ((tid & 63) == 0) red[tid >> 6] = s;
  __syncthreads();
  float mean = (red[0] + red[1] + red[2] + red[3]) * (1.0f / D);
  __syncthreads();
  float da = a - mean, db = b - mean;
  float v = wave_sum(da * da + db * db);
  if ((tid & 63) == 0) red[tid >> 6] = v;
  __syncthreads();
  float var = (red[0] + red[1] + red[2] + red[3]) * (1.0f / D);
  float m = rbf(mean), vv = rbf(var);
  float sd = rbf(sqrtf(rbf(vv + 1e-5f)));
  xn[tid] = rbf(rbf(a - m) / sd);
  xn[tid + 256] = rbf(rbf(b - m) / sd);
  __syncthreads();
}

__global__ void __launch_bounds__(256) k_pro(const unsigned short* x, const unsigned short* wq, const unsigned short* w1,
                                             const unsigned short* b1, float* q, float* hbuf) {
  __shared__ float xn[D];
  __shared__ float red[4];
  __shared__ float part[16][128];
  layernorm(x, xn, red);
  const int tid = threadIdx.x, c = tid & 15, r = tid >> 4;
  const bool isq = blockIdx.x < 32;
  const unsigned short* w = isq ? wq + blockIdx.x * 128 : w1 + (blockIdx.x - 32) * 128;
  const int ld = isq ? 4096 : F;
  float acc[8] = {0, 0, 0, 0, 0, 0, 0, 0};
  for (int i = r; i < D; i += 16) {
    u32x4 v = *(const u32x4*)(w + (size_t)i * ld + c * 8);
    float xv = xn[i];
#pragma unroll
    for (int k = 0; k < 4; ++k) {
      acc[2 * k] = fmaf(xv, __uint_as_float(v[k] << 16), acc[2 * k]);
      acc[2 * k + 1] = fmaf(xv, __uint_as_float(v[k] & 0xffff0000u), acc[2 * k + 1]);
    }
  }
#pragma unroll
  for (int k = 0; k < 8; ++k) part[r][c * 8 + k] = acc[k];
  __syncthreads();
  if (tid < 128) {
    float s = 0;
    for (int i = 0; i < 16; ++i) s += part[i][tid];
    if (isq) {
      q[blockIdx.x * 128 + tid] = rbf(s);
    } else {
      int col = (blockIdx.x - 32) * 128 + tid;
      float hh = rbf(rbf(s) + bf2f(b1[col]));
      hh = hh > 0.f ? hh : 0.f;
      hbuf[col] = rbf(hh * hh);
    }
  }
}

__global__ void __launch_bounds__(256) k_attn(const unsigned short* __restrict__ K, const unsigned short* __restrict__ V,
                                              const float* __restrict__ q, float* __restrict__ part) {
  __shared__ float mrg[16][132];
  const int h = blockIdx.x / NSPLIT, s = blockIdx.x % NSPLIT;
  const int tid = threadIdx.x, lane = tid & 63, wave = tid >> 6, r = lane >> 4, c = lane & 15;
  u32 qp[4];
#pragma unroll
  for (int k = 0; k < 4; ++k) {
    float lo = q[h * 128 + c * 8 + 2 * k], hi = q[h * 128 + c * 8 + 2 * k + 1];
    qp[k] = (__float_as_uint(lo) >> 16) | (__float_as_uint(hi) & 0xffff0000u);
  }
  const size_t base = ((size_t)h * SK + (size_t)s * KEYS_PER_SPLIT) * DH;
  const unsigned short* kb = K + base + (size_t)(wave * 16 + r) * DH + c * 8;
  const unsigned short* vb = V + base + (size_t)(wave * 16 + r) * DH + c * 8;
  float m = -INFINITY, l = 0.f, acc[8] = {0, 0, 0, 0, 0, 0, 0, 0};
  constexpr int NT = KEYS_PER_SPLIT / 64;
  u32x4 Kn[4], Vn[4];
#pragma unroll
  for (int t = 0; t < 4; ++t) {
    Kn[t] = __builtin_nontemporal_load((const u32x4*)(kb + t * 4 * DH));
    Vn[t] = __builtin_nontemporal_load((const u32x4*)(vb + t * 4 * DH));
  }
  for (int i = 0; i < NT; ++i) {
    u32x4 Kc[4], Vc[4];
#pragma unroll
    for (int t = 0; t < 4; ++t) { Kc[t] = Kn[t]; Vc[t] = Vn[t]; }
    if (i + 1 < NT) {
#pragma unroll
      for (int t = 0; t < 4; ++t) {
        Kn[t] = __builtin_nontemporal_load((const u32x4*)(kb + (size_t)(i + 1) * 64 * DH + t * 4 * DH));
        Vn[t] = __builtin_nontemporal_load((const u32x4*)(vb + (size_t)(i + 1) * 64 * DH + t * 4 * DH));
      }
    }
    float sc[4];
#pragma unroll
    for (int t = 0; t < 4; ++t) {
      float d = 0.f;
#pragma unroll
      for (int k = 0; k < 4; ++k) d = dot2(Kc[t][k], qp[k], d);
      d = row16_sum(d);
      d = rbf(rbf(d) * 0.08838834764831845f);
      sc[t] = d * 1.4426950408889634f;
    }
    float mn = fmaxf(fmaxf(fmaxf(sc[0], sc[1]), fmaxf(sc[2], sc[3])), m);
    float alpha = __builtin_amdgcn_exp2f(m - mn);
    m = mn;
    float p[4];
#pragma unroll
    for (int t = 0; t < 4; ++t) p[t] = __builtin_amdgcn_exp2f(sc[t] - mn);
    l = l * alpha + ((p[0] + p[1]) + (p[2] + p[3]));
#pragma unroll
    for (int k = 0; k < 8; ++k) acc[k] *= alpha;
#pragma unroll
    for (int t = 0; t < 4; ++t) {
#pragma unroll
      for (int k = 0; k < 4; ++k) {
        acc[2 * k] = fmaf(p[t], __uint_as_float(Vc[t][k] << 16), acc[2 * k]);
        acc[2 * k + 1] = fmaf(p[t], __uint_as_float(Vc[t][k] & 0xffff0000u), acc[2 * k + 1]);
      }
    }
  }
  const int row = wave * 4 + r;
#pragma unroll
  for (int k = 0; k < 8; ++k) mrg[row][4 + c * 8 + k] = acc[k];
  if (c == 0) { mrg[row][0] = m; mrg[row][1] = l; }
  __syncthreads();
  if (tid < 128) {
    float M = -INFINITY;
    for (int i = 0; i < 16; ++i) M = fmaxf(M, mrg[i][0]);
    float L = 0.f, o = 0.f;
    for (int i = 0; i < 16; ++i) {
      float w = __builtin_amdgcn_exp2f(mrg[i][0] - M);
      L = fmaf(w, mrg[i][1], L);
      o = fmaf(w, mrg[i][4 + tid], o);
    }
    float* pp = part + (size_t)blockIdx.x * 132;
    pp[4 + tid] = o;
    if (tid == 0) { pp[0] = M; pp[1] = L; }
  }
}

__global__ void __launch_bounds__(256) k_epi(const float* __restrict__ part, const float* __restrict__ hbuf,
                                             const unsigned short* __restrict__ wo, const unsigned short* __restrict__ w2,
                                             const unsigned short* __restrict__ b2, const unsigned short* __restrict__ x,
                                             __hip_bfloat16* __restrict__ out) {
  __shared__ float wgt[H * NSPLIT];
  __shared__ float red[2][4][8];
  const int tid = threadIdx.x;
  for (int i = tid; i < H * NSPLIT; i += 256) {
    int h = i / NSPLIT;
    float M = -INFINITY;
    for (int s = 0; s < NSPLIT; ++s) M = fmaxf(M, part[(size_t)(h * NSPLIT + s) * 132]);
    float L = 0.f;
    for (int s = 0; s < NSPLIT; ++s)
      L = fmaf(__builtin_amdgcn_exp2f(part[(size_t)(h * NSPLIT + s) * 132] - M), part[(size_t)(h * NSPLIT + s) * 132 + 1], L);
    wgt[i] = __builtin_amdgcn_exp2f(part[(size_t)i * 132] - M) / L;
  }
  __syncthreads();
  const int col0 = blockIdx.x * 8;
  float a[8] = {0, 0, 0, 0, 0, 0, 0, 0}, b[8] = {0, 0, 0, 0, 0, 0, 0, 0};
  for (int i = tid; i < H * DH; i += 256) {
    int h = i >> 7, d = i & 127;
    float o = 0.f;
    for (int s = 0; s < NSPLIT; ++s) o = fmaf(wgt[h * NSPLIT + s], part[(size_t)(h * NSPLIT + s) * 132 + 4 + d], o);
    o = rbf(o);
    u32x4 v = *(const u32x4*)(wo + (size_t)i * D + col0);
#pragma unroll
    for (int k = 0; k < 4; ++k) {
      a[2 * k] = fmaf(o, __uint_as_float(v[k] << 16), a[2 * k]);
      a[2 * k + 1] = fmaf(o, __uint_as_float(v[k] & 0xffff0000u), a[2 * k + 1]);
    }
  }
  for (int i = tid; i < F; i += 256) {
    float hv = hbuf[i];
    u32x4 v = *(const u32x4*)(w2 + (size_t)i * D + col0);
#pragma unroll
    for (int k = 0; k < 4; ++k) {
      b[2 * k] = fmaf(hv, __uint_as_float(v[k] << 16), b[2 * k]);
      b[2 * k + 1] = fmaf(hv, __uint_as_float(v[k] & 0xffff0000u), b[2 * k + 1]);
    }
  }
#pragma unroll
  for (int k = 0; k < 8; ++k) { a[k] = wave_sum(a[k]); b[k] = wave_sum(b[k]); }
  if ((tid & 63) == 0) {
#pragma unroll
    for (int k = 0; k < 8; ++k) { red[0][tid >> 6][k] = a[k]; red[1][tid >> 6][k] = b[k]; }
  }
  __syncthreads();
  if (tid < 8) {
    float A = red[0][0][tid] + red[0][1][tid] + red[0][2][tid] + red[0][3][tid];
    float B = red[1][0][tid] + red[1][1][tid] + red[1][2][tid] + red[1][3][tid];
    float xv = bf2f(x[col0 + tid]), bb = bf2f(b2[col0 + tid]);
    float res = rbf(rbf(xv + rbf(A)) + rbf(rbf(B) + bb));
    out[col0 + tid] = __float2bfloat16(res);
  }
}

extern "C" void kernel_launch(void* const* d_in, const int* in_sizes, int n_in, void* d_out, int out_size, void* d_ws,
                              size_t ws_size, hipStream_t stream) {
  const unsigned short* x = (const unsigned short*)d_in[0];
  const unsigned short* K = (const unsigned short*)d_in[1];
  const unsigned short* V = (const unsigned short*)d_in[2];
  const unsigned short* wq = (const unsigned short*)d_in[3];
  const unsigned short* wo = (const unsigned short*)d_in[4];
  const unsigned short* w1 = (const unsigned short*)d_in[6];
  const unsigned short* b1 = (const unsigned short*)d_in[7];
  const unsigned short* w2 = (const unsigned short*)d_in[8];
  const unsigned short* b2 = (const unsigned short*)d_in[9];
  float* q = (float*)d_ws;
  float* hbuf = q + 4096;
  float* part = hbuf + 2048;
  k_pro<<<32 + 14, 256, 0, stream>>>(x, wq, w1, b1, q, hbuf);
  k_attn<<<H * NSPLIT, 256, 0, stream>>>(K, V, q, part);
  k_epi<<<64, 256, 0, stream>>>(part, hbuf, wo, w2, b2, x, (__hip_bfloat16*)d_out);
}
```

```cpp
#include <hip/hip_runtime.h>
#include <stdint.h>

#define CFG_U 2
#define CFG_D 1
#define CFG_NX 1
#define CFG_WO 2
#define CFG_ORDER 1

typedef unsigned int u32;
typedef unsigned long long u64;
typedef unsigned short u16;
typedef u32 u32x4 __attribute__((ext_vector_type(4)));
typedef float f32x4 __attribute__((ext_vector_type(4)));
typedef float f32x2 __attribute__((ext_vector_type(2)));
typedef __bf16 bf16x2 __attribute__((ext_vector_type(2)));
typedef __amdgpu_buffer_rsrc_t rsrc_t;

constexpr int DM = 512, NH = 32, SK = 65536, DH = 128, FF = 1792;
constexpr int NWG = 256, NW = 8, NTHR = NW * 64;
constexpr int CHUNK = SK / 8 * DH * 2;
constexpr int WS_GEN = 0;
constexpr int WS_ML = 4096;
constexpr int WS_A = 8192;
constexpr int WS_B = WS_A + NWG * 4096;
constexpr int WS_Q = WS_B + NWG * 4096;
constexpr int WS_BYTES = WS_Q + NWG * 2048;
constexpr int AUX_NT = 2, AUX_SC1 = 16;

__device__ __forceinline__ rsrc_t mkrsrc(const void* p, int bytes) {
  return __builtin_amdgcn_make_buffer_rsrc((void*)p, 0, bytes, 0x00020000);
}
__device__ __forceinline__ float asf(u32 x) { return __builtin_bit_cast(float, x); }
__device__ __forceinline__ u32 asu(float x) { return __builtin_bit_cast(u32, x); }
__device__ __forceinline__ float lo16(u32 x) { return asf(x << 16); }
__device__ __forceinline__ float hi16(u32 x) { return asf(x & 0xffff0000u); }
__device__ __forceinline__ float rbf(float x) { __bf16 b = (__bf16)x; return (float)b; }
__device__ __forceinline__ float dot2(u32 a, u32 b, float acc) {
  return __builtin_amdgcn_fdot2_f32_bf16(__builtin_bit_cast(bf16x2, a), __builtin_bit_cast(bf16x2, b), acc, false);
}
template <int CTRL> __device__ __forceinline__ float dpp(float x) {
  return __builtin_bit_cast(float, __builtin_amdgcn_mov_dpp(__builtin_bit_cast(int, x), CTRL, 0xf, 0xf, true));
}
constexpr int XOR1 = 0xB1, XOR2 = 0x4E, XOR7 = 0x141, XOR8 = 0x128;
__device__ __forceinline__ float row16_sum(float x) {
  x += dpp<XOR8>(x); x += dpp<XOR7>(x); x += dpp<XOR2>(x); x += dpp<XOR1>(x); return x;
}
__device__ __forceinline__ float xrow_sum(float x) {
  auto s = __builtin_amdgcn_permlane16_swap(asu(x), asu(x), false, false);
  x = asf(s[0]) + asf(s[1]);
  auto t = __builtin_amdgcn_permlane32_swap(asu(x), asu(x), false, false);
  return asf(t[0]) + asf(t[1]);
}
__device__ __forceinline__ float xrow_max(float x) {
  auto s = __builtin_amdgcn_permlane16_swap(asu(x), asu(x), false, false);
  x = fmaxf(asf(s[0]), asf(s[1]));
  auto t = __builtin_amdgcn_permlane32_swap(asu(x), asu(x), false, false);
  return fmaxf(asf(t[0]), asf(t[1]));
}
__device__ __forceinline__ float wave_sum(float x) { return xrow_sum(row16_sum(x)); }
__device__ __forceinline__ void lds_barrier() {
  asm volatile("s_waitcnt lgkmcnt(0)" ::: "memory");
  __builtin_amdgcn_s_barrier();
  asm volatile("" ::: "memory");
}
#define BLD(r, vo, so, aux) __builtin_amdgcn_raw_buffer_load_b128(r, vo, so, aux)
#define BST(v, r, vo, aux) __builtin_amdgcn_raw_buffer_store_b128(v, r, vo, 0, aux)
#define BAD(v) (((v)[1] ^ tlo) | ((v)[3] ^ thi))

#define STAMP(i) do { } while (0)
#define FLUSH() do { } while (0)

__global__ void __launch_bounds__(NTHR, 2)
fused_kernel(const u16* x, const u16* K, const u16* V, const u16* wq, const u16* wo, const u16* w1, const u16* b1,
             const u16* w2, const u16* b2, u16* out, unsigned char* ws
) {
  constexpr int U = CFG_U, D = CFG_D, R = D + 1, ORDER = CFG_ORDER, NX = CFG_NX;
  constexpr int TILE = U * 1024, NT = CHUNK / (TILE * NW);
  __shared__ __attribute__((aligned(16))) float lds[8 * 512 + 8 * 132 + 64 + 256 + (CFG_WO == 2 ? 32768 : 0)];
  float* const l_y = lds;
  float* const l_acc = lds + 8 * 512;
  float* const l_h = l_acc + 8 * 132;
  float* const l_fin = l_h + 64;
  float* const l_wo = l_fin + 256;

  const int wg = blockIdx.x, tid = threadIdx.x, lane = tid & 63;
  const int wave = __builtin_amdgcn_readfirstlane(tid >> 6);
  const int r = lane >> 4, c = lane & 15;
  const int h = (wg & 7) * 4 + (wg >> 6), s = (wg >> 3) & 7;
  const int pid = h * 8 + s;
  STAMP(0);

  const rsrc_t rk = mkrsrc((const char*)K + (size_t)pid * CHUNK, CHUNK);
  const rsrc_t rv = mkrsrc((const char*)V + (size_t)pid * CHUNK, CHUNK);
  const rsrc_t rw = mkrsrc(ws, WS_BYTES);
  const rsrc_t rx = mkrsrc(x, DM * 2);
  const bool ffn = pid < FF / 8;
  const rsrc_t r1 = mkrsrc(w1, ffn ? DM * FF * 2 : 0);
  const rsrc_t r2 = mkrsrc(w2, ffn ? FF * DM * 2 : 0);
  const rsrc_t rb1 = mkrsrc(b1, ffn ? FF * 2 : 0);
  const rsrc_t rq = mkrsrc(wq, DM * NH * DH * 2);
  const rsrc_t ro = mkrsrc(wo, NH * DH * DM * 2);

  const int row0 = s * 64 + wave * 8;
  const u32x4 xs = BLD(rx, lane * 16, 0, 0);
  const u32 xq = __builtin_amdgcn_raw_buffer_load_b32(rx, r * 4, row0 * 2, 0);
  const u32 xt = __builtin_amdgcn_raw_buffer_load_b16(rx, (lane >> 3) * 2, row0 * 2, 0);
  const u32x4 wq0 = BLD(rq, r * 2 * (NH * DH * 2) + c * 16, row0 * (NH * DH * 2) + h * 256, 0);
  const u32x4 wq1 = BLD(rq, r * 2 * (NH * DH * 2) + c * 16, (row0 + 1) * (NH * DH * 2) + h * 256, 0);
  const u32x4 w1v = BLD(r1, (lane >> 3) * (FF * 2) + (lane & 7) * 16, row0 * (FF * 2) + h * 128, 0);
  const auto gen2 = __builtin_amdgcn_raw_buffer_load_b64(rw, 0, WS_GEN, AUX_SC1);
  asm volatile("" ::: "memory");
  const int vo = lane * 16;
  u32x4 kr[R][U], vr[R][U], kx[NX + 1][U], vx[NX + 1][U];
#define ISSUE(kk, vv, t) do { const int so_ = (wave + (t) * NW) * TILE; _Pragma("unroll") for (int u = 0; u < U; ++u) kk[u] = BLD(rk, vo + u * 1024, so_, AUX_NT); \
    _Pragma("unroll") for (int u = 0; u < U; ++u) vv[u] = BLD(rv, vo + u * 1024, so_, AUX_NT); asm volatile("" ::: "memory"); } while (0)
#pragma unroll
  for (int d = 0; d < R; ++d) ISSUE(kr[d], vr[d], d);
  __builtin_amdgcn_sched_barrier(0);
  STAMP(7);

  float mean_b, rstd;
  {
    float e[8];
#pragma unroll
    for (int k = 0; k < 4; ++k) { e[2 * k] = lo16(xs[k]); e[2 * k + 1] = hi16(xs[k]); }
    float sum = ((e[0] + e[1]) + (e[2] + e[3])) + ((e[4] + e[5]) + (e[6] + e[7]));
    const float mean = wave_sum(sum) * (1.0f / DM);
    float sq = 0.f;
#pragma unroll
    for (int k = 0; k < 8; ++k) { const float d = e[k] - mean; sq = fmaf(d, d, sq); }
    const float var = wave_sum(sq) * (1.0f / DM);
    mean_b = rbf(mean);
    const float sd = rbf(__builtin_amdgcn_sqrtf(rbf(rbf(var) + 1e-5f)));
    rstd = __builtin_amdgcn_rcpf(sd);
  }
  STAMP(8);
  {
    const float x0 = rbf(rbf(lo16(xq) - mean_b) * rstd), x1 = rbf(rbf(hi16(xq) - mean_b) * rstd);
    const float xh = rbf(rbf(lo16(xt) - mean_b) * rstd);
    float a[8], p[8];
#pragma unroll
    for (int k = 0; k < 4; ++k) {
      a[2 * k] = xrow_sum(fmaf(x1, lo16(wq1[k]), x0 * lo16(wq0[k])));
      a[2 * k + 1] = xrow_sum(fmaf(x1, hi16(wq1[k]), x0 * hi16(wq0[k])));
    }
    if (r == 0) {
      *(f32x4*)(l_y + wave * 128 + c * 8) = f32x4{a[0], a[1], a[2], a[3]};
      *(f32x4*)(l_y + wave * 128 + c * 8 + 4) = f32x4{a[4], a[5], a[6], a[7]};
    }
#pragma unroll
    for (int k = 0; k < 4; ++k) {
      float t0 = xh * lo16(w1v[k]), t1 = xh * hi16(w1v[k]);
      t0 += dpp<XOR8>(t0); t1 += dpp<XOR8>(t1);
      p[2 * k] = xrow_sum(t0); p[2 * k + 1] = xrow_sum(t1);
    }
    if (lane < 8) {
      *(f32x4*)(l_y + 1024 + wave * 64 + lane * 8) = f32x4{p[0], p[1], p[2], p[3]};
      *(f32x4*)(l_y + 1024 + wave * 64 + lane * 8 + 4) = f32x4{p[4], p[5], p[6], p[7]};
    }
  }
  const u64 gen = ((u64)(u32)__builtin_amdgcn_readfirstlane(gen2[1]) << 32) | (u32)__builtin_amdgcn_readfirstlane(gen2[0]);
  const u64 tag = gen * 6364136223846793005ull + 1442695040888963407ull;
  const u32 tlo = (u32)tag, thi = (u32)(tag >> 32);
  STAMP(9);
  lds_barrier();
  if (tid < 96) {
    const float* src = tid < 64 ? l_y + tid * 2 : l_y + 1024 + (tid - 64) * 2;
    const int st = tid < 64 ? 128 : 64;
    f32x2 t = *(const f32x2*)src;
#pragma unroll
    for (int w = 1; w < 8; ++w) t += *(const f32x2*)(src + w * st);
    BST((u32x4{asu(t[0]), tlo, asu(t[1]), thi}), rw, WS_Q + pid * 2048 + tid * 16, AUX_SC1);
  }
  STAMP(10);
  u32 qp[4];
  u32x4 w2v; u32 b1v;
  {
    const int sob = WS_Q + (h * 8 + wave) * 2048;
    u32x4 gq = BLD(rw, vo, sob, AUX_SC1);
    u32x4 gh = BLD(rw, 1024 + s * 64 + (lane & 3) * 16, sob, AUX_SC1);
    asm volatile("" ::: "memory");
    w2v = BLD(r2, (lane >> 3) * (DM * 2) + (lane & 7) * 16, pid * 8 * (DM * 2) + wave * 128, 0);
    b1v = __builtin_amdgcn_raw_buffer_load_b16(rb1, (lane >> 3) * 2, pid * 16, 0);
#pragma unroll
    for (int e = 0; e < NX; ++e) ISSUE(kx[e], vx[e], R + e);
    __builtin_amdgcn_sched_barrier(0);
    if (__builtin_amdgcn_ballot_w64((BAD(gq) | BAD(gh)) != 0) != 0) {
      do {
        __builtin_amdgcn_s_sleep(1);
        gq = BLD(rw, vo, sob, AUX_SC1);
        gh = BLD(rw, 1024 + s * 64 + (lane & 3) * 16, sob, AUX_SC1);
        asm volatile("" : "+v"(gq), "+v"(gh));
      } while (__builtin_amdgcn_ballot_w64((BAD(gq) | BAD(gh)) != 0) != 0);
    }
    *(f32x2*)(l_y + 2048 + wave * 128 + lane * 2) = f32x2{asf(gq[0]), asf(gq[2])};
    if (lane < 4) *(f32x2*)(l_h + wave * 8 + lane * 2) = f32x2{asf(gh[0]), asf(gh[2])};
    STAMP(1);
    lds_barrier();
    const float* lq = l_y + 2048;
    const f32x4 p0 = *(const f32x4*)(lq + (2 * r) * 128 + c * 8), p1 = *(const f32x4*)(lq + (2 * r) * 128 + c * 8 + 4);
    const f32x4 p2 = *(const f32x4*)(lq + (2 * r + 1) * 128 + c * 8), p3 = *(const f32x4*)(lq + (2 * r + 1) * 128 + c * 8 + 4);
    float qf[8];
#pragma unroll
    for (int k = 0; k < 4; ++k) { qf[k] = xrow_sum(p0[k] + p2[k]); qf[4 + k] = xrow_sum(p1[k] + p3[k]); }
#pragma unroll
    for (int k = 0; k < 4; ++k) qp[k] = (asu(rbf(qf[2 * k])) >> 16) | asu(rbf(qf[2 * k + 1]));
  }
  STAMP(2);
  float m = -__builtin_inff(), l = 0.f, acc[8] = {0, 0, 0, 0, 0, 0, 0, 0};
#define TOUCH(kk, vv) do { if (ORDER == 1) { _Pragma("unroll") for (int u = 0; u < U; ++u) asm volatile("" : "+v"(kk[u]), "+v"(vv[u])); } } while (0)
#define CONSUME(kk, vv) do { \
    float sc[U]; \
    _Pragma("unroll") for (int u = 0; u < U; ++u) { \
      float d = dot2(kk[u][0], qp[0], 0.f); d = dot2(kk[u][1], qp[1], d); d = dot2(kk[u][2], qp[2], d); d = dot2(kk[u][3], qp[3], d); \
      d = row16_sum(d); \
      sc[u] = rbf(rbf(d) * 0.08838834764831845f) * 1.4426950408889634f; } \
    float mn = m; \
    _Pragma("unroll") for (int u = 0; u < U; ++u) mn = fmaxf(mn, sc[u]); \
    const float alpha = __builtin_amdgcn_exp2f(m - mn); \
    m = mn; l *= alpha; \
    _Pragma("unroll") for (int k = 0; k < 8; ++k) acc[k] *= alpha; \
    _Pragma("unroll") for (int u = 0; u < U; ++u) { \
      const float p = __builtin_amdgcn_exp2f(sc[u] - mn); l += p; \
      _Pragma("unroll") for (int k = 0; k < 4; ++k) { acc[2 * k] = fmaf(p, lo16(vv[u][k]), acc[2 * k]); acc[2 * k + 1] = fmaf(p, hi16(vv[u][k]), acc[2 * k + 1]); } } \
    asm volatile("" : "+v"(acc[0]), "+v"(acc[1]), "+v"(acc[2]), "+v"(acc[3]), "+v"(acc[4]), "+v"(acc[5]), "+v"(acc[6]), "+v"(acc[7]), "+v"(l), "+v"(m)); \
  } while (0)
  CONSUME(kr[0], vr[0]);
  __builtin_amdgcn_sched_barrier(0);
  ISSUE(kr[0], vr[0], NX + R);
  u32x4 wv[16];
  if (CFG_WO == 1) {
#pragma unroll
    for (int j = 0; j < 16; ++j) wv[j] = BLD(ro, vo + (j & 3) * (DM * 2), (h * DH + wave * 16 + (j & 12)) * (DM * 2), 0);
  } else if (CFG_WO == 0) {
#pragma unroll
    for (int j = 0; j < 16; ++j) wv[j] = u32x4{qp[0] + j, qp[1], qp[2], qp[3]};
  }
  asm volatile("" ::: "memory");
  __builtin_amdgcn_sched_barrier(0);
  {
    float hs = l_h[(lane & 7) * 8 + (lane >> 3)];
    hs += dpp<XOR1>(hs); hs += dpp<XOR2>(hs); hs += dpp<XOR7>(hs);
    float hh = rbf(rbf(hs) + lo16(b1v));
    hh = hh > 0.f ? hh : 0.f;
    hh = rbf(hh * hh);
    float p[8];
#pragma unroll
    for (int k = 0; k < 4; ++k) {
      float t0 = hh * lo16(w2v[k]), t1 = hh * hi16(w2v[k]);
      t0 += dpp<XOR8>(t0); t1 += dpp<XOR8>(t1);
      p[2 * k] = xrow_sum(t0); p[2 * k + 1] = xrow_sum(t1);
    }
    const bool s0 = lane & 8, s1 = lane & 16;
    const float e0 = s0 ? p[2] : p[0], e1 = s0 ? p[6] : p[4], o0 = s0 ? p[3] : p[1], o1 = s0 ? p[7] : p[5];
    const float y0 = s1 ? e1 : e0, y1 = s1 ? o1 : o0;
    const int so = (lane < 32 && ffn) ? WS_B + pid * 4096 + wave * 512 + ((lane & 7) * 4 + ((lane >> 3) & 3)) * 16 : WS_BYTES;
    BST((u32x4{asu(y0), tlo, asu(y1), thi}), rw, so, AUX_SC1);
  }
  __builtin_amdgcn_sched_barrier(0);
#pragma unroll
  for (int e = 0; e < NX; ++e) CONSUME(kx[e], vx[e]);
  CONSUME(kr[1], vr[1]);
  STAMP(6);

  constexpr int NL = NT - NX - 2;
  int i = 0;
  if (CFG_WO == 2) {
    static_assert(16 % R == 0, "");
    for (; i < 16; i += R) {
#pragma unroll
      for (int ph = 0; ph < R; ++ph) {
        TOUCH(kr[(ph + 2) % R], vr[(ph + 2) % R]);
        __builtin_amdgcn_raw_ptr_buffer_load_lds(ro, (__attribute__((address_space(3))) void*)(l_wo + (wave * 16 + i + ph) * 256), 16, vo,
                                                 (h * DH + wave * 16 + i + ph) * (DM * 2), 0, 0);
        ISSUE(kr[(ph + 1) % R], vr[(ph + 1) % R], NX + R + 1 + i + ph);
        CONSUME(kr[(ph + 2) % R], vr[(ph + 2) % R]);
        __builtin_amdgcn_sched_barrier(0);
      }
    }
  }
  for (; i < NL / R * R; i += R) {
#pragma unroll
    for (int ph = 0; ph < R; ++ph) {
      TOUCH(kr[(ph + 2) % R], vr[(ph + 2) % R]);
      ISSUE(kr[(ph + 1) % R], vr[(ph + 1) % R], NX + R + 1 + i + ph);
      CONSUME(kr[(ph + 2) % R], vr[(ph + 2) % R]);
      __builtin_amdgcn_sched_barrier(0);
    }
  }
#pragma unroll
  for (int ph = 0; ph < NL % R; ++ph) CONSUME(kr[(ph + 2) % R], vr[(ph + 2) % R]);
  STAMP(3);
  if (CFG_WO == 2) {
#pragma unroll
    for (int j = 0; j < 16; ++j) wv[j] = *(const u32x4*)(l_wo + (wave * 16 + j) * 256 + lane * 4);
  }

  {
    const float mw = xrow_max(m);
    const float f = __builtin_amdgcn_exp2f(m - mw);
    l = xrow_sum(l * f);
#pragma unroll
    for (int k = 0; k < 8; ++k) acc[k] = xrow_sum(acc[k] * f);
    if (r == 0) {
      *(f32x4*)(l_acc + wave * 132 + c * 8) = f32x4{acc[0], acc[1], acc[2], acc[3]};
      *(f32x4*)(l_acc + wave * 132 + c * 8 + 4) = f32x4{acc[4], acc[5], acc[6], acc[7]};
      if (c == 0) *(f32x2*)(l_acc + wave * 132 + 128) = f32x2{mw, l};
    }
  }
  lds_barrier();
  STAMP(11);
  float Mg, Lg;
  {
    f32x2 ml[8];
#pragma unroll
    for (int w = 0; w < 8; ++w) ml[w] = *(const f32x2*)(l_acc + w * 132 + 128);
    float ov[8];
#pragma unroll
    for (int w = 0; w < 8; ++w) ov[w] = l_acc[w * 132 + wave * 16 + c];
    Mg = ml[0][0];
#pragma unroll
    for (int w = 1; w < 8; ++w) Mg = fmaxf(Mg, ml[w][0]);
    float o = 0.f; Lg = 0.f;
#pragma unroll
    for (int w = 0; w < 8; ++w) { const float f = __builtin_amdgcn_exp2f(ml[w][0] - Mg); Lg = fmaf(f, ml[w][1], Lg); o = fmaf(f, ov[w], o); }
    float y[8] = {0, 0, 0, 0, 0, 0, 0, 0};
#pragma unroll
    for (int j = 0; j < 16; ++j) {
      const float oj = asf(__builtin_amdgcn_readlane(asu(o), j));
#pragma unroll
      for (int k = 0; k < 4; ++k) { y[2 * k] = fmaf(oj, lo16(wv[j][k]), y[2 * k]); y[2 * k + 1] = fmaf(oj, hi16(wv[j][k]), y[2 * k + 1]); }
    }
    *(f32x4*)(l_y + wave * 512 + lane * 8) = f32x4{y[0], y[1], y[2], y[3]};
    *(f32x4*)(l_y + wave * 512 + lane * 8 + 4) = f32x4{y[4], y[5], y[6], y[7]};
  }
  lds_barrier();
  if (tid < 256) {
    f32x2 t = *(const f32x2*)(l_y + tid * 2);
#pragma unroll
    for (int w = 1; w < 8; ++w) t += *(const f32x2*)(l_y + w * 512 + tid * 2);
    BST((u32x4{asu(t[0]), tlo, asu(t[1]), thi}), rw, WS_A + pid * 4096 + tid * 16, AUX_SC1);
  } else if (tid == 256) {
    BST((u32x4{asu(Mg), tlo, asu(Lg), thi}), rw, WS_ML + pid * 16 + (tid - 256), AUX_SC1);
  }
  STAMP(4);
  if (s != 0) { FLUSH(); return; }

  {
    const u32 xo = __builtin_amdgcn_raw_buffer_load_b16(rx, (tid & 15) * 2, h * 32, 0);
    const u32 bo = __builtin_amdgcn_raw_buffer_load_b16(mkrsrc(b2, DM * 2), (tid & 15) * 2, h * 32, 0);
    u32x4 pa[4], pm[4], pb[4];
    const int voa = (lane >> 3) * 4096 + (lane & 7) * 16, vom = (lane >> 3) * 16;
    const u32x4 okv = {0u, tlo, 0u, thi};
#pragma unroll
    for (int j = 0; j < 4; ++j) { pa[j] = u32x4{0u, ~tlo, 0u, 0u}; pm[j] = pa[j]; pb[j] = wave < 7 ? pa[j] : okv; }
    while (true) {
#pragma unroll
      for (int j = 0; j < 4; ++j) {
        const int hp = wave * 4 + j;
        if (BAD(pa[j])) pa[j] = BLD(rw, voa, WS_A + hp * 32768 + h * 128, AUX_SC1);
        if (BAD(pm[j])) pm[j] = BLD(rw, vom, WS_ML + hp * 128, AUX_SC1);
        if (BAD(pb[j])) pb[j] = BLD(rw, voa, WS_B + hp * 32768 + h * 128, AUX_SC1);
      }
      u32 bad = 0;
#pragma unroll
      for (int j = 0; j < 4; ++j) bad |= BAD(pa[j]) | BAD(pm[j]) | BAD(pb[j]);
      if (__builtin_amdgcn_ballot_w64(bad != 0) == 0) break;
      __builtin_amdgcn_s_sleep(8);
    }
    STAMP(12);
    float a0 = 0.f, a1 = 0.f, c0 = 0.f, c1 = 0.f;
#pragma unroll
    for (int j = 0; j < 4; ++j) {
      const float mj = asf(pm[j][0]);
      const float M = xrow_max(fmaxf(mj, dpp<XOR8>(mj)));
      const float e = __builtin_amdgcn_exp2f(mj - M);
      float L = e * asf(pm[j][2]);
      L += dpp<XOR8>(L); L = xrow_sum(L);
      const float w = e / L;
      a0 = fmaf(w, asf(pa[j][0]), a0); a1 = fmaf(w, asf(pa[j][2]), a1);
      c0 += asf(pb[j][0]); c1 += asf(pb[j][2]);
    }
    a0 += dpp<XOR8>(a0); a1 += dpp<XOR8>(a1); c0 += dpp<XOR8>(c0); c1 += dpp<XOR8>(c1);
    a0 = xrow_sum(a0); a1 = xrow_sum(a1); c0 = xrow_sum(c0); c1 = xrow_sum(c1);
    if (lane < 8) {
      *(f32x2*)(l_fin + wave * 32 + lane * 2) = f32x2{a0, a1};
      *(f32x2*)(l_fin + wave * 32 + 16 + lane * 2) = f32x2{c0, c1};
    }
    lds_barrier();
    if (tid < 16) {
      float A = 0.f, B = 0.f;
#pragma unroll
      for (int w = 0; w < 8; ++w) { A += l_fin[w * 32 + tid]; B += l_fin[w * 32 + 16 + tid]; }
      const float res = rbf(rbf(lo16(xo) + rbf(A)) + rbf(rbf(B) + lo16(bo)));
      out[h * 16 + tid] = (u16)(asu(res) >> 16);
    }
    if (h == 0 && tid == 64) __hip_atomic_store((u64*)(ws + WS_GEN), tag, __ATOMIC_RELAXED, __HIP_MEMORY_SCOPE_AGENT);
    STAMP(5);
    FLUSH();
  }
}

extern "C" void kernel_launch(void* const* d_in, const int* in_sizes, int n_in, void* d_out, int out_size, void* d_ws,
                              size_t ws_size, hipStream_t stream) {
  fused_kernel<<<NWG, NTHR, 0, stream>>>((const u16*)d_in[0], (const u16*)d_in[1], (const u16*)d_in[2], (const u16*)d_in[3],
                                         (const u16*)d_in[4], (const u16*)d_in[6], (const u16*)d_in[7], (const u16*)d_in[8],
                                         (const u16*)d_in[9], (u16*)d_out, (unsigned char*)d_ws);
}
```

```cpp
#include <hip/hip_runtime.h>
#include <stdint.h>

#define CFG_NSTAT 109
#define CFG_SLEEP 1

typedef unsigned int u32;
typedef unsigned long long u64;
typedef unsigned short u16;
typedef u32 u32x4 __attribute__((ext_vector_type(4)));
typedef float f32x4 __attribute__((ext_vector_type(4)));
typedef float f32x2 __attribute__((ext_vector_type(2)));
typedef __bf16 bf16x2 __attribute__((ext_vector_type(2)));
typedef __amdgpu_buffer_rsrc_t rsrc_t;

constexpr int DM = 512, NH = 32, SK = 65536, DH = 128, FF = 1792;
constexpr int NWG = 256, NW = 8, NTHR = NW * 64;
constexpr int CHUNK = SK / 8 * DH * 2;
constexpr int WS_GEN = 0;
constexpr int WS_CT = 8192;
constexpr int WS_B = 16384;
constexpr int WS_Q = WS_B + NWG * 4096;
constexpr int WS_O = WS_Q + NWG * 2048;
constexpr int WS_Y = WS_O + NWG * 2048;
constexpr int WS_BYTES = WS_Y + NWG * 512;
constexpr int AUX_NT = 2, AUX_SC1 = 16;

__device__ __forceinline__ rsrc_t mkrsrc(const void* p, int bytes) {
  return __builtin_amdgcn_make_buffer_rsrc((void*)p, 0, bytes, 0x00020000);
}
__device__ __forceinline__ float asf(u32 x) { return __builtin_bit_cast(float, x); }
__device__ __forceinline__ u32 asu(float x) { return __builtin_bit_cast(u32, x); }
__device__ __forceinline__ float lo16(u32 x) { return asf(x << 16); }
__device__ __forceinline__ float hi16(u32 x) { return asf(x & 0xffff0000u); }
__device__ __forceinline__ float rbf(float x) { __bf16 b = (__bf16)x; return (float)b; }
__device__ __forceinline__ u32 pk2(float a, float b) { return __builtin_bit_cast(u32, bf16x2{(__bf16)a, (__bf16)b}); }
__device__ __forceinline__ float dot2(u32 a, u32 b, float acc) {
  return __builtin_amdgcn_fdot2_f32_bf16(__builtin_bit_cast(bf16x2, a), __builtin_bit_cast(bf16x2, b), acc, false);
}
template <int CTRL> __device__ __forceinline__ float dpp(float x) {
  return __builtin_bit_cast(float, __builtin_amdgcn_mov_dpp(__builtin_bit_cast(int, x), CTRL, 0xf, 0xf, true));
}
constexpr int XOR1 = 0xB1, XOR2 = 0x4E, XOR7 = 0x141, XOR8 = 0x128;
__device__ __forceinline__ float row16_sum(float x) {
  x += dpp<XOR8>(x); x += dpp<XOR7>(x); x += dpp<XOR2>(x); x += dpp<XOR1>(x); return x;
}
__device__ __forceinline__ float xrow_sum(float x) {
  auto s = __builtin_amdgcn_permlane16_swap(asu(x), asu(x), false, false);
  x = asf(s[0]) + asf(s[1]);
  auto t = __builtin_amdgcn_permlane32_swap(asu(x), asu(x), false, false);
  return asf(t[0]) + asf(t[1]);
}
__device__ __forceinline__ float xrow_max(float x) {
  auto s = __builtin_amdgcn_permlane16_swap(asu(x), asu(x), false, false);
  x = fmaxf(asf(s[0]), asf(s[1]));
  auto t = __builtin_amdgcn_permlane32_swap(asu(x), asu(x), false, false);
  return fmaxf(asf(t[0]), asf(t[1]));
}
__device__ __forceinline__ float hi8_sum(float x) { x += dpp<XOR8>(x); return xrow_sum(x); }
__device__ __forceinline__ float wave_sum(float x) { return xrow_sum(row16_sum(x)); }
__device__ __forceinline__ void lds_barrier() {
  asm volatile("s_waitcnt lgkmcnt(0)" ::: "memory");
  __builtin_amdgcn_s_barrier();
  asm volatile("" ::: "memory");
}
#define BLD(r, vo, so, aux) __builtin_amdgcn_raw_buffer_load_b128(r, vo, so, aux)
#define BST(v, r, vo, aux) __builtin_amdgcn_raw_buffer_store_b128(v, r, vo, 0, aux)
#define BAD(v) (((v)[1] ^ tlo) | ((v)[3] ^ thi))
#define ANY(x) (__builtin_amdgcn_ballot_w64((x) != 0) != 0)
#define SB() __builtin_amdgcn_sched_barrier(0)

#define STAMP(i) do { } while (0)
#define FLUSH() do { } while (0)

__global__ void __launch_bounds__(NTHR, 2)
fused_kernel(const u16* x, const u16* K, const u16* V, const u16* wq, const u16* wo, const u16* w1, const u16* b1,
             const u16* w2, const u16* b2, u16* out, unsigned char* ws
) {
  constexpr int U = 2, NB = 5, NSTAT = CFG_NSTAT;
  constexpr int TILE = U * 1024, NT = CHUNK / (TILE * NW), NGRP = (NT - NSTAT) * NW / 2 * 8;
  static_assert((NSTAT - NB) % 2 == 0, "");
  __shared__ __attribute__((aligned(16))) float lds[24 * 1024];
  float* const l_y = lds;
  float* const l_acc = lds + 8 * 512;
  float* const l_h = l_acc + 8 * 132;
  float* const l_fin = l_h + 64;

  const int wg = blockIdx.x, tid = threadIdx.x, lane = tid & 63;
  const int wave = __builtin_amdgcn_readfirstlane(tid >> 6);
  const int r = lane >> 4, c = lane & 15, hi = lane >> 3, lo = lane & 7;
  const int h = wg >> 3, s = wg & 7;
  STAMP(S_start);

  const rsrc_t rk = mkrsrc((const char*)K + (size_t)h * (8 * CHUNK), 8 * CHUNK);
  const rsrc_t rv = mkrsrc((const char*)V + (size_t)h * (8 * CHUNK), 8 * CHUNK);
  const rsrc_t rw = mkrsrc(ws, WS_BYTES);
  const rsrc_t rx = mkrsrc(x, DM * 2);
  const bool ffn = wg < FF / 8;
  const rsrc_t r1 = mkrsrc(w1, ffn ? DM * FF * 2 : 0);
  const rsrc_t r2 = mkrsrc(w2, ffn ? FF * DM * 2 : 0);
  const rsrc_t rb1 = mkrsrc(b1, ffn ? FF * 2 : 0);
  const rsrc_t rq = mkrsrc(wq, DM * NH * DH * 2);
  const rsrc_t ro = mkrsrc(wo, NH * DH * DM * 2);

  const int row0 = s * 64 + wave * 8;
  const u32x4 xs = BLD(rx, lane * 16, 0, 0);
  const u32 xq = __builtin_amdgcn_raw_buffer_load_b32(rx, r * 4, row0 * 2, 0);
  const u32 xt = __builtin_amdgcn_raw_buffer_load_b16(rx, hi * 2, row0 * 2, 0);
  const u32x4 wq0 = BLD(rq, r * 2 * (NH * DH * 2) + c * 16, row0 * (NH * DH * 2) + h * 256, 0);
  const u32x4 wq1 = BLD(rq, r * 2 * (NH * DH * 2) + c * 16, (row0 + 1) * (NH * DH * 2) + h * 256, 0);
  const u32x4 w1v = BLD(r1, hi * (FF * 2) + lo * 16, row0 * (FF * 2) + h * 128, 0);
  const auto gen2 = __builtin_amdgcn_raw_buffer_load_b64(rw, 0, WS_GEN, AUX_SC1);
  u32 ct0 = 0;
  if (s == 0 && tid == 0) ct0 = __hip_atomic_exchange((u32*)(ws + WS_CT + h * 256), 0u, __ATOMIC_RELAXED, __HIP_MEMORY_SCOPE_AGENT);
  asm volatile("" ::: "memory");
  __builtin_amdgcn_s_barrier();
  asm volatile("" ::: "memory");
  STAMP(S_bar0);
  const int vo = lane * 16, sb = s * CHUNK + wave * TILE;
  u32x4 kb[NB][U], vb[NB][U];
#define ISSUE(sl, so_) do { _Pragma("unroll") for (int u = 0; u < U; ++u) kb[sl][u] = BLD(rk, vo + u * 1024, so_, AUX_NT); \
    _Pragma("unroll") for (int u = 0; u < U; ++u) vb[sl][u] = BLD(rv, vo + u * 1024, so_, AUX_NT); asm volatile("" ::: "memory"); } while (0)
#define TOUCH(sl) do { _Pragma("unroll") for (int u = 0; u < U; ++u) asm volatile("" : "+v"(kb[sl][u]), "+v"(vb[sl][u]) :: "memory"); } while (0)
#define ST(t) (sb + (t) * (NW * TILE))
  ISSUE(0, ST(0));
  SB();
  STAMP(S_issued);

  float mean_b, rstd;
  {
    float e[8];
#pragma unroll
    for (int k = 0; k < 4; ++k) { e[2 * k] = lo16(xs[k]); e[2 * k + 1] = hi16(xs[k]); }
    float sum = ((e[0] + e[1]) + (e[2] + e[3])) + ((e[4] + e[5]) + (e[6] + e[7]));
    const float mean = wave_sum(sum) * (1.0f / DM);
    float sq = 0.f;
#pragma unroll
    for (int k = 0; k < 8; ++k) { const float d = e[k] - mean; sq = fmaf(d, d, sq); }
    const float var = wave_sum(sq) * (1.0f / DM);
    mean_b = rbf(mean);
    const float sd = rbf(__builtin_amdgcn_sqrtf(rbf(rbf(var) + 1e-5f)));
    rstd = __builtin_amdgcn_rcpf(sd);
  }
  STAMP(S_ln);
  {
    const float x0 = rbf(rbf(lo16(xq) - mean_b) * rstd), x1 = rbf(rbf(hi16(xq) - mean_b) * rstd);
    const float xh = rbf(rbf(lo16(xt) - mean_b) * rstd);
    float a[8], p[8];
#pragma unroll
    for (int k = 0; k < 4; ++k) {
      a[2 * k] = xrow_sum(fmaf(x1, lo16(wq1[k]), x0 * lo16(wq0[k])));
      a[2 * k + 1] = xrow_sum(fmaf(x1, hi16(wq1[k]), x0 * hi16(wq0[k])));
    }
    if (r == 0) {
      *(f32x4*)(l_y + wave * 128 + c * 8) = f32x4{a[0], a[1], a[2], a[3]};
      *(f32x4*)(l_y + wave * 128 + c * 8 + 4) = f32x4{a[4], a[5], a[6], a[7]};
    }
#pragma unroll
    for (int k = 0; k < 4; ++k) { p[2 * k] = hi8_sum(xh * lo16(w1v[k])); p[2 * k + 1] = hi8_sum(xh * hi16(w1v[k])); }
    if (lane < 8) {
      *(f32x4*)(l_y + 1024 + wave * 64 + lane * 8) = f32x4{p[0], p[1], p[2], p[3]};
      *(f32x4*)(l_y + 1024 + wave * 64 + lane * 8 + 4) = f32x4{p[4], p[5], p[6], p[7]};
    }
  }
  const u64 gen = ((u64)(u32)__builtin_amdgcn_readfirstlane(gen2[1]) << 32) | (u32)__builtin_amdgcn_readfirstlane(gen2[0]);
  const u64 tag = gen * 6364136223846793005ull + 1442695040888963407ull;
  const u32 tlo = (u32)tag, thi = (u32)(tag >> 32);
  asm volatile("" :: "v"(ct0));
  STAMP(S_part);
  SB();
  TOUCH(0);
  STAMP(S_t0);
  ISSUE(1, ST(1));
  SB();
  lds_barrier();
  {
    const float* src = wave == 0 ? l_y + lane * 2 : l_y + 1024 + (lane & 31) * 2;
    const int st = wave == 0 ? 128 : 64;
    f32x2 t = *(const f32x2*)src;
#pragma unroll
    for (int w = 1; w < 8; ++w) t += *(const f32x2*)(src + w * st);
    BST((u32x4{asu(t[0]), tlo, asu(t[1]), thi}), rw, tid < 96 ? WS_Q + wg * 2048 + tid * 16 : WS_BYTES, AUX_SC1);
  }
  STAMP(S_qpub);
  SB();
  TOUCH(1);
  STAMP(S_t1);
  ISSUE(2, ST(2));
  const u32x4 w2v = BLD(r2, hi * (DM * 2) + lo * 16, wg * 8 * (DM * 2) + wave * 128, 0);
  const u32 b1v = __builtin_amdgcn_raw_buffer_load_b16(rb1, hi * 2, wg * 16, 0);
  SB();
  u32 qp[4];
  {
    const int sob = WS_Q + (h * 8 + wave) * 2048;
    TOUCH(2);
    STAMP(S_t2);
    u32x4 gq = BLD(rw, vo, sob, AUX_SC1);
    u32x4 gh = BLD(rw, 1024 + s * 64 + (lane & 3) * 16, sob, AUX_SC1);
    asm volatile("" ::: "memory");
    ISSUE(3, ST(3));
    SB();
    if (ANY(BAD(gq) | BAD(gh))) {
      do {
        __builtin_amdgcn_s_sleep(1);
        gq = BLD(rw, vo, sob, AUX_SC1);
        gh = BLD(rw, 1024 + s * 64 + (lane & 3) * 16, sob, AUX_SC1);
        asm volatile("" : "+v"(gq), "+v"(gh));
      } while (ANY(BAD(gq) | BAD(gh)));
    }
    *(f32x2*)(l_y + 2048 + wave * 128 + lane * 2) = f32x2{asf(gq[0]), asf(gq[2])};
    if (lane < 4) *(f32x2*)(l_h + wave * 8 + lane * 2) = f32x2{asf(gh[0]), asf(gh[2])};
    STAMP(S_gath);
    SB();
    TOUCH(3);
    STAMP(S_t3);
    ISSUE(4, ST(4));
    SB();
    lds_barrier();
    const float* lq = l_y + 2048;
    const f32x4 p0 = *(const f32x4*)(lq + (2 * r) * 128 + c * 8), p1 = *(const f32x4*)(lq + (2 * r) * 128 + c * 8 + 4);
    const f32x4 p2 = *(const f32x4*)(lq + (2 * r + 1) * 128 + c * 8), p3 = *(const f32x4*)(lq + (2 * r + 1) * 128 + c * 8 + 4);
    float qf[8];
#pragma unroll
    for (int k = 0; k < 4; ++k) { qf[k] = xrow_sum(p0[k] + p2[k]); qf[4 + k] = xrow_sum(p1[k] + p3[k]); }
#pragma unroll
    for (int k = 0; k < 4; ++k) qp[k] = pk2(qf[2 * k], qf[2 * k + 1]);
  }
  STAMP(S_qready);
  float m = -__builtin_inff(), l = 0.f;
  f32x2 acc[4] = {{0, 0}, {0, 0}, {0, 0}, {0, 0}};
#define CONSUME(sl) do { \
    const u32x4* kk = kb[sl]; const u32x4* vv = vb[sl]; \
    float sc[U]; \
    _Pragma("unroll") for (int u = 0; u < U; ++u) { \
      float d = dot2(kk[u][0], qp[0], 0.f); d = dot2(kk[u][1], qp[1], d); d = dot2(kk[u][2], qp[2], d); d = dot2(kk[u][3], qp[3], d); \
      d = row16_sum(d); \
      sc[u] = rbf(rbf(d) * 0.08838834764831845f) * 1.4426950408889634f; } \
    float mn = m; \
    _Pragma("unroll") for (int u = 0; u < U; ++u) mn = fmaxf(mn, sc[u]); \
    const float alpha = __builtin_amdgcn_exp2f(m - mn); \
    m = mn; l *= alpha; \
    _Pragma("unroll") for (int k = 0; k < 4; ++k) acc[k] *= f32x2{alpha, alpha}; \
    _Pragma("unroll") for (int u = 0; u < U; ++u) { \
      const float p = __builtin_amdgcn_exp2f(sc[u] - mn); l += p; \
      _Pragma("unroll") for (int k = 0; k < 4; ++k) acc[k] = __builtin_elementwise_fma(f32x2{p, p}, f32x2{lo16(vv[u][k]), hi16(vv[u][k])}, acc[k]); } \
    asm volatile("" : "+v"(acc[0]), "+v"(acc[1]), "+v"(acc[2]), "+v"(acc[3]), "+v"(l), "+v"(m)); \
  } while (0)
  CONSUME(0);
  CONSUME(1);
  SB();
  TOUCH(4);
  STAMP(S_t4);
  ISSUE(0, ST(5));
  const u32x4 woa = BLD(ro, lo * 2 * (DM * 2) + hi * 16, (h * DH + wave * 16) * (DM * 2) + s * 128, 0);
  const u32x4 wob = BLD(ro, lo * 2 * (DM * 2) + hi * 16, (h * DH + wave * 16 + 1) * (DM * 2) + s * 128, 0);
  SB();
  CONSUME(2);
  CONSUME(3);
  CONSUME(4);
  SB();
  TOUCH(0);
  STAMP(S_t5);
  ISSUE(1, ST(6));
  SB();
  {
    float hs = l_h[lo * 8 + hi];
    hs += dpp<XOR1>(hs); hs += dpp<XOR2>(hs); hs += dpp<XOR7>(hs);
    float hh = rbf(rbf(hs) + lo16(b1v));
    hh = hh > 0.f ? hh : 0.f;
    hh = rbf(hh * hh);
    float p[8];
#pragma unroll
    for (int k = 0; k < 4; ++k) { p[2 * k] = hi8_sum(hh * lo16(w2v[k])); p[2 * k + 1] = hi8_sum(hh * hi16(w2v[k])); }
    const bool s0 = lane & 8, s1 = lane & 16;
    const float e0 = s0 ? p[2] : p[0], e1 = s0 ? p[6] : p[4], o0 = s0 ? p[3] : p[1], o1 = s0 ? p[7] : p[5];
    const float y0 = s1 ? e1 : e0, y1 = s1 ? o1 : o0;
    const int so = (lane < 32 && ffn) ? WS_B + wg * 4096 + wave * 512 + (lo * 4 + (hi & 3)) * 16 : WS_BYTES;
    BST((u32x4{asu(y0), tlo, asu(y1), thi}), rw, so, AUX_SC1);
  }
  f32x2 wa[4], wb[4];
#pragma unroll
  for (int k = 0; k < 4; ++k) { wa[k] = f32x2{lo16(woa[k]), hi16(woa[k])}; wb[k] = f32x2{lo16(wob[k]), hi16(wob[k])}; }
  asm volatile("" : "+v"(wa[0]), "+v"(wa[1]), "+v"(wa[2]), "+v"(wa[3]), "+v"(wb[0]), "+v"(wb[1]), "+v"(wb[2]), "+v"(wb[3]));
  SB();
  CONSUME(0);
  SB();

  for (int t = NB + 2; t < NSTAT - 2; t += 2) {
#pragma unroll
    for (int ph = 0; ph < 2; ++ph) {
      TOUCH(ph ^ 1);
      ISSUE(ph, ST(t + ph));
      CONSUME(ph ^ 1);
      SB();
    }
  }
  STAMP(S_statend);
  {
    int cvo = WS_CT + h * 256;
    asm volatile("" : "+v"(cvo));
    int o0 = ST(NSTAT - 2), o1 = ST(NSTAT - 1);
    while (true) {
      TOUCH(1);
      ISSUE(0, o0);
      CONSUME(1);
      SB();
      TOUCH(0);
      int tk = 0;
      if (lane == 0) tk = __builtin_amdgcn_raw_ptr_buffer_atomic_add_i32(1, rw, cvo, 0, 0);
      asm volatile("" ::: "memory");
      ISSUE(1, o1);
      CONSUME(0);
      SB();
      const u32 g = __builtin_amdgcn_readfirstlane(tk);
      if (g >= (u32)NGRP) break;
      o0 = (g & 7) * CHUNK + NSTAT * (NW * TILE) + (g >> 3) * (2 * TILE);
      o1 = o0 + TILE;
    }
  }
  const bool fin = s == (h & 7), finb = fin && wave < 7;
  const rsrc_t rwb = mkrsrc(ws, finb ? WS_BYTES : 0);
  u32x4 pb[4];
#pragma unroll
  for (int j = 0; j < 4; ++j) pb[j] = BLD(rwb, hi * 4096 + lo * 16, WS_B + (wave * 4 + j) * 32768 + h * 128, AUX_SC1);
  const u32 xo = __builtin_amdgcn_raw_buffer_load_b32(rx, lo * 4, h * 32, 0);
  const u32 bo = __builtin_amdgcn_raw_buffer_load_b32(mkrsrc(b2, DM * 2), lo * 4, h * 32, 0);
  asm volatile("" ::: "memory");
  SB();
  CONSUME(1);
  STAMP(S_loopend);

  {
    const float mw = xrow_max(m);
    const float f = __builtin_amdgcn_exp2f(m - mw);
    l = xrow_sum(l * f);
    float a[8];
#pragma unroll
    for (int k = 0; k < 4; ++k) { a[2 * k] = xrow_sum(acc[k][0] * f); a[2 * k + 1] = xrow_sum(acc[k][1] * f); }
    if (r == 0) {
      *(f32x4*)(l_acc + wave * 132 + c * 8) = f32x4{a[0], a[1], a[2], a[3]};
      *(f32x4*)(l_acc + wave * 132 + c * 8 + 4) = f32x4{a[4], a[5], a[6], a[7]};
      if (c == 0) *(f32x2*)(l_acc + wave * 132 + 128) = f32x2{mw, l};
    }
  }
  lds_barrier();
  STAMP(S_bar2);
  if (wave == 0) {
    f32x2 ml[8], ov[8];
#pragma unroll
    for (int w = 0; w < 8; ++w) { ml[w] = *(const f32x2*)(l_acc + w * 132 + 128); ov[w] = *(const f32x2*)(l_acc + w * 132 + lane * 2); }
    float Mg = ml[0][0], Lg = 0.f;
#pragma unroll
    for (int w = 1; w < 8; ++w) Mg = fmaxf(Mg, ml[w][0]);
    f32x2 o = {0, 0};
#pragma unroll
    for (int w = 0; w < 8; ++w) {
      const float f = __builtin_amdgcn_exp2f(ml[w][0] - Mg);
      Lg = fmaf(f, ml[w][1], Lg);
      o = __builtin_elementwise_fma(f32x2{f, f}, ov[w], o);
    }
    BST((u32x4{asu(o[0]), tlo, asu(o[1]), thi}), rw, WS_O + wg * 2048 + vo, AUX_SC1);
    BST((u32x4{asu(Mg), tlo, asu(Lg), thi}), rw, lane == 0 ? WS_O + wg * 2048 + 1024 : WS_BYTES, AUX_SC1);
    STAMP(S_opub);
  }
  {
    const int sob = WS_O + h * 8 * 2048;
    u32x4 go = BLD(rw, hi * 2048 + lo * 16, sob + wave * 128, AUX_SC1);
    u32x4 gm = BLD(rw, hi * 2048, sob + 1024, AUX_SC1);
    asm volatile("" ::: "memory");
    SB();
    if (finb && ANY(BAD(pb[0]) | BAD(pb[1]) | BAD(pb[2]) | BAD(pb[3]))) {
      do {
        __builtin_amdgcn_s_sleep(1);
#pragma unroll
        for (int j = 0; j < 4; ++j) pb[j] = BLD(rwb, hi * 4096 + lo * 16, WS_B + (wave * 4 + j) * 32768 + h * 128, AUX_SC1);
        asm volatile("" : "+v"(pb[0]), "+v"(pb[1]), "+v"(pb[2]), "+v"(pb[3]) :: "memory");
      } while (ANY(BAD(pb[0]) | BAD(pb[1]) | BAD(pb[2]) | BAD(pb[3])));
    }
    if (fin) {
      const float c0 = hi8_sum((asf(pb[0][0]) + asf(pb[1][0])) + (asf(pb[2][0]) + asf(pb[3][0])));
      const float c1 = hi8_sum((asf(pb[0][2]) + asf(pb[1][2])) + (asf(pb[2][2]) + asf(pb[3][2])));
      if (lane < 8) *(f32x2*)(l_fin + wave * 16 + lane * 2) = f32x2{c0, c1};
    }
    SB();
    while (ANY(BAD(go) | BAD(gm))) {
      __builtin_amdgcn_s_sleep(CFG_SLEEP);
      go = BLD(rw, hi * 2048 + lo * 16, sob + wave * 128, AUX_SC1);
      gm = BLD(rw, hi * 2048, sob + 1024, AUX_SC1);
      asm volatile("" ::: "memory");
    }
    STAMP(S_opoll);
    const float mj = asf(gm[0]);
    const float M = xrow_max(fmaxf(mj, dpp<XOR8>(mj)));
    const float e = __builtin_amdgcn_exp2f(mj - M);
    const float L = hi8_sum(e * asf(gm[2]));
    const float rl = __builtin_amdgcn_rcpf(L);
    const float oa = hi8_sum(e * asf(go[0])) * rl, ob = hi8_sum(e * asf(go[2])) * rl;
    float y[8];
#pragma unroll
    for (int k = 0; k < 4; ++k) {
      const f32x2 t = __builtin_elementwise_fma(f32x2{ob, ob}, wb[k], f32x2{oa, oa} * wa[k]);
      y[2 * k] = t[0]; y[2 * k + 1] = t[1];
    }
#pragma unroll
    for (int k = 0; k < 8; ++k) { y[k] += dpp<XOR1>(y[k]); y[k] += dpp<XOR2>(y[k]); y[k] += dpp<XOR7>(y[k]); }
    if (lo == 0) {
      *(f32x4*)(l_y + wave * 64 + hi * 8) = f32x4{y[0], y[1], y[2], y[3]};
      *(f32x4*)(l_y + wave * 64 + hi * 8 + 4) = f32x4{y[4], y[5], y[6], y[7]};
    }
  }
  lds_barrier();
  STAMP(S_bar3);
  if (wave == 0) {
    const float* src = l_y + (lane >> 5) * 256 + (lane & 31) * 2;
    const f32x2 t = (*(const f32x2*)src + *(const f32x2*)(src + 64)) + (*(const f32x2*)(src + 128) + *(const f32x2*)(src + 192));
    const auto u0 = __builtin_amdgcn_permlane32_swap(asu(t[0]), asu(t[0]), false, false);
    const auto u1 = __builtin_amdgcn_permlane32_swap(asu(t[1]), asu(t[1]), false, false);
    BST((u32x4{asu(asf(u0[0]) + asf(u0[1])), tlo, asu(asf(u1[0]) + asf(u1[1])), thi}), rw, lane < 32 ? WS_Y + wg * 512 + vo : WS_BYTES, AUX_SC1);
    STAMP(S_ypub);
  } else if (wave == 1 && fin) {
    u32x4 py[4];
    const int voy = hi * 4096 + lo * 16, soy = WS_Y + (h >> 2) * 512 + (h & 3) * 128;
#pragma unroll
    for (int j = 0; j < 4; ++j) py[j] = BLD(rw, voy, soy + j * 32768, AUX_SC1);
    asm volatile("" ::: "memory");
    SB();
    f32x2 B = *(const f32x2*)(l_fin + lo * 2);
#pragma unroll
    for (int w = 1; w < 7; ++w) B += *(const f32x2*)(l_fin + w * 16 + lo * 2);
    float B0 = rbf(rbf(B[0]) + lo16(bo)), B1 = rbf(rbf(B[1]) + hi16(bo));
    asm volatile("" : "+v"(B0), "+v"(B1));
    SB();
    while (ANY(BAD(py[0]) | BAD(py[1]) | BAD(py[2]) | BAD(py[3]))) {
      __builtin_amdgcn_s_sleep(CFG_SLEEP);
#pragma unroll
      for (int j = 0; j < 4; ++j) py[j] = BLD(rw, voy, soy + j * 32768, AUX_SC1);
      asm volatile("" ::: "memory");
    }
    STAMP(S_ypoll);
    const float A0 = hi8_sum((asf(py[0][0]) + asf(py[1][0])) + (asf(py[2][0]) + asf(py[3][0])));
    const float A1 = hi8_sum((asf(py[0][2]) + asf(py[1][2])) + (asf(py[2][2]) + asf(py[3][2])));
    const u32 res = pk2(rbf(lo16(xo) + rbf(A0)) + B0, rbf(hi16(xo) + rbf(A1)) + B1);
    if (lane < 8) ((u32*)out)[h * 8 + lane] = res;
    if (h == 0 && lane == 8) __hip_atomic_store((u64*)(ws + WS_GEN), tag, __ATOMIC_RELAXED, __HIP_MEMORY_SCOPE_AGENT);
    STAMP(S_final);
  }
  FLUSH();
}

extern "C" void kernel_launch(void* const* d_in, const int* in_sizes, int n_in, void* d_out, int out_size, void* d_ws,
                              size_t ws_size, hipStream_t stream) {
  fused_kernel<<<NWG, NTHR, 0, stream>>>((const u16*)d_in[0], (const u16*)d_in[1], (const u16*)d_in[2], (const u16*)d_in[3],
                                         (const u16*)d_in[4], (const u16*)d_in[6], (const u16*)d_in[7], (const u16*)d_in[8],
                                         (const u16*)d_in[9], (u16*)d_out, (unsigned char*)d_ws);
}
```

```cpp
#include <hip/hip_runtime.h>
#include <stdint.h>

#define CFG_NS 57
#define CFG_SLEEP 1

typedef unsigned int u32;
typedef unsigned long long u64;
typedef unsigned short u16;
typedef u32 u32x4 __attribute__((ext_vector_type(4)));
typedef float f32x4 __attribute__((ext_vector_type(4)));
typedef float f32x2 __attribute__((ext_vector_type(2)));
typedef __bf16 bf16x2 __attribute__((ext_vector_type(2)));
typedef __amdgpu_buffer_rsrc_t rsrc_t;

constexpr int DM = 512, NH = 32, SK = 65536, DH = 128, FF = 1792;
constexpr int NWG = 256, NW = 8, NTHR = NW * 64;
constexpr int CHUNK = SK / 8 * DH * 2;
constexpr int WS_GEN = 0;
constexpr int WS_CT = 8192;
constexpr int WS_B = 16384;
constexpr int WS_Q = WS_B + NWG * 512;
constexpr int WS_O = WS_Q + NWG * 2048;
constexpr int WS_Y = WS_O + NWG * 2048;
constexpr int WS_BYTES = WS_Y + NWG * 512;
constexpr int AUX_NT = 2, AUX_SC1 = 16;

__device__ __forceinline__ rsrc_t mkrsrc(const void* p, int bytes) {
  return __builtin_amdgcn_make_buffer_rsrc((void*)p, 0, bytes, 0x00020000);
}
__device__ __forceinline__ float asf(u32 x) { return __builtin_bit_cast(float, x); }
__device__ __forceinline__ u32 asu(float x) { return __builtin_bit_cast(u32, x); }
__device__ __forceinline__ float lo16(u32 x) { return asf(x << 16); }
__device__ __forceinline__ float hi16(u32 x) { return asf(x & 0xffff0000u); }
__device__ __forceinline__ float rbf(float x) { __bf16 b = (__bf16)x; return (float)b; }
__device__ __forceinline__ u32 pk2(float a, float b) { return __builtin_bit_cast(u32, bf16x2{(__bf16)a, (__bf16)b}); }
__device__ __forceinline__ float dot2(u32 a, u32 b, float acc) {
  return __builtin_amdgcn_fdot2_f32_bf16(__builtin_bit_cast(bf16x2, a), __builtin_bit_cast(bf16x2, b), acc, false);
}
template <int CTRL> __device__ __forceinline__ float dpp(float x) {
  return __builtin_bit_cast(float, __builtin_amdgcn_mov_dpp(__builtin_bit_cast(int, x), CTRL, 0xf, 0xf, true));
}
constexpr int XOR1 = 0xB1, XOR2 = 0x4E, XOR7 = 0x141, XOR8 = 0x128, ROR4 = 0x124;
__device__ __forceinline__ float row16_sum(float x) {
  x += dpp<XOR8>(x); x += dpp<XOR7>(x); x += dpp<XOR2>(x); x += dpp<XOR1>(x); return x;
}
__device__ __forceinline__ float xrow_sum(float x) {
  auto s = __builtin_amdgcn_permlane16_swap(asu(x), asu(x), false, false);
  x = asf(s[0]) + asf(s[1]);
  auto t = __builtin_amdgcn_permlane32_swap(asu(x), asu(x), false, false);
  return asf(t[0]) + asf(t[1]);
}
__device__ __forceinline__ float xrow_max(float x) {
  auto s = __builtin_amdgcn_permlane16_swap(asu(x), asu(x), false, false);
  x = fmaxf(asf(s[0]), asf(s[1]));
  auto t = __builtin_amdgcn_permlane32_swap(asu(x), asu(x), false, false);
  return fmaxf(asf(t[0]), asf(t[1]));
}
__device__ __forceinline__ float hi8_sum(float x) { x += dpp<XOR8>(x); return xrow_sum(x); }
__device__ __forceinline__ float wave_sum(float x) { return xrow_sum(row16_sum(x)); }
__device__ __forceinline__ void lds_barrier() {
  asm volatile("s_waitcnt lgkmcnt(0)" ::: "memory");
  __builtin_amdgcn_s_barrier();
  asm volatile("" ::: "memory");
}
#define BLD(r, vo, so, aux) __builtin_amdgcn_raw_buffer_load_b128(r, vo, so, aux)
#define BST(v, r, vo, aux) __builtin_amdgcn_raw_buffer_store_b128(v, r, vo, 0, aux)
#define BAD(v) (((v)[1] ^ tlo) | ((v)[3] ^ thi))
#define ANY(x) (__builtin_amdgcn_ballot_w64((x) != 0) != 0)
#define SB() __builtin_amdgcn_sched_barrier(0)

#define STAMP(i) do { } while (0)
#define FLUSH() do { } while (0)

__global__ void __launch_bounds__(NTHR, 2)
fused_kernel(const u16* x, const u16* K, const u16* V, const u16* wq, const u16* wo, const u16* w1, const u16* b1,
             const u16* w2, const u16* b2, u16* out, unsigned char* ws
) {
  constexpr int U = 4, NB = 5, NS = CFG_NS, FG = FF / NH;
  constexpr int TILE = U * 1024, NT = CHUNK / (TILE * NW), NGRP = (NT - NS) * NW * 8;
  static_assert(NS % 2 == 1 && NS >= 11 && NS < NT, "");
  __shared__ __attribute__((aligned(16))) float lds[24 * 1024];
  float* const l_y = lds;
  float* const l_acc = lds + 8 * 512;
  float* const l_h = l_acc + 8 * 132;
  float* const l_b = l_h + 512;
  float* const l_s = l_b + 512;
  u32* const l_cnt = (u32*)(l_s + 8 * NT * 16);

  const int wg = blockIdx.x, tid = threadIdx.x, lane = tid & 63;
  const int wave = __builtin_amdgcn_readfirstlane(tid >> 6);
  const int r = lane >> 4, c = lane & 15, hi = lane >> 3, lo = lane & 7;
  const int h = wg >> 3, s = wg & 7;
  STAMP(S_start);

  const rsrc_t rk = mkrsrc((const char*)K + (size_t)h * (8 * CHUNK), 8 * CHUNK);
  const rsrc_t rv = mkrsrc((const char*)V + (size_t)h * (8 * CHUNK), 8 * CHUNK);
  const rsrc_t rw = mkrsrc(ws, WS_BYTES);
  const rsrc_t rx = mkrsrc(x, DM * 2);
  const rsrc_t r1 = mkrsrc(w1, DM * FF * 2);
  const rsrc_t r2 = mkrsrc(w2, FF * DM * 2);
  const rsrc_t rb1 = mkrsrc(b1, FF * 2);
  const rsrc_t rq = mkrsrc(wq, DM * NH * DH * 2);
  const rsrc_t ro = mkrsrc(wo, NH * DH * DM * 2);

  const int row0 = s * 64 + wave * 8;
  const u32x4 xs = BLD(rx, lane * 16, 0, 0);
  const u32 xq = __builtin_amdgcn_raw_buffer_load_b32(rx, r * 4, row0 * 2, 0);
  const u32 xt = __builtin_amdgcn_raw_buffer_load_b16(rx, hi * 2, row0 * 2, 0);
  const u32x4 wq0 = BLD(rq, r * 2 * (NH * DH * 2) + c * 16, row0 * (NH * DH * 2) + h * 256, 0);
  const u32x4 wq1 = BLD(rq, r * 2 * (NH * DH * 2) + c * 16, (row0 + 1) * (NH * DH * 2) + h * 256, 0);
  const u32x4 w1v = BLD(r1, lo < 7 ? hi * (FF * 2) + lo * 16 : DM * FF * 2, row0 * (FF * 2) + h * (FG * 2), 0);
  const auto gen2 = __builtin_amdgcn_raw_buffer_load_b64(rw, 0, WS_GEN, AUX_SC1);
  u32 ct0 = 0;
  if (s == 0 && tid == 0) ct0 = __hip_atomic_exchange((u32*)(ws + WS_CT + h * 256), 0u, __ATOMIC_RELAXED, __HIP_MEMORY_SCOPE_AGENT);
  if (tid == 0) *l_cnt = 0;
  asm volatile("" ::: "memory");
  __builtin_amdgcn_s_barrier();
  asm volatile("" ::: "memory");
  STAMP(S_bar0);
  const int vo = lane * 16, sb = s * CHUNK + wave * TILE;
  u32x4 tb[NB][U];
#define ISSUE(sl, rs_, so_) do { _Pragma("unroll") for (int u = 0; u < U; ++u) tb[sl][u] = BLD(rs_, vo + u * 1024, so_, AUX_NT); asm volatile("" ::: "memory"); } while (0)
#define TOUCH(sl) do { _Pragma("unroll") for (int u = 0; u < U; ++u) asm volatile("" : "+v"(tb[sl][u]) :: "memory"); } while (0)
#define ST(t) (sb + (t) * (NW * TILE))
  ISSUE(0, rk, ST(0));
  SB();
  STAMP(S_issued);

  float mean_b, rstd;
  {
    float e[8];
#pragma unroll
    for (int k = 0; k < 4; ++k) { e[2 * k] = lo16(xs[k]); e[2 * k + 1] = hi16(xs[k]); }
    float sum = ((e[0] + e[1]) + (e[2] + e[3])) + ((e[4] + e[5]) + (e[6] + e[7]));
    const float mean = wave_sum(sum) * (1.0f / DM);
    float sq = 0.f;
#pragma unroll
    for (int k = 0; k < 8; ++k) { const float d = e[k] - mean; sq = fmaf(d, d, sq); }
    const float var = wave_sum(sq) * (1.0f / DM);
    mean_b = rbf(mean);
    const float sd = rbf(__builtin_amdgcn_sqrtf(rbf(rbf(var) + 1e-5f)));
    rstd = __builtin_amdgcn_rcpf(sd);
  }
  STAMP(S_ln);
  {
    const float x0 = rbf(rbf(lo16(xq) - mean_b) * rstd), x1 = rbf(rbf(hi16(xq) - mean_b) * rstd);
    const float xh = rbf(rbf(lo16(xt) - mean_b) * rstd);
    float a[8], p[8];
#pragma unroll
    for (int k = 0; k < 4; ++k) {
      a[2 * k] = xrow_sum(fmaf(x1, lo16(wq1[k]), x0 * lo16(wq0[k])));
      a[2 * k + 1] = xrow_sum(fmaf(x1, hi16(wq1[k]), x0 * hi16(wq0[k])));
    }
    if (r == 0) {
      *(f32x4*)(l_y + wave * 128 + c * 8) = f32x4{a[0], a[1], a[2], a[3]};
      *(f32x4*)(l_y + wave * 128 + c * 8 + 4) = f32x4{a[4], a[5], a[6], a[7]};
    }
#pragma unroll
    for (int k = 0; k < 4; ++k) { p[2 * k] = hi8_sum(xh * lo16(w1v[k])); p[2 * k + 1] = hi8_sum(xh * hi16(w1v[k])); }
    if (lane < 8) {
      *(f32x4*)(l_y + 1024 + wave * 64 + lane * 8) = f32x4{p[0], p[1], p[2], p[3]};
      *(f32x4*)(l_y + 1024 + wave * 64 + lane * 8 + 4) = f32x4{p[4], p[5], p[6], p[7]};
    }
  }
  const u64 gen = ((u64)(u32)__builtin_amdgcn_readfirstlane(gen2[1]) << 32) | (u32)__builtin_amdgcn_readfirstlane(gen2[0]);
  const u64 tag = gen * 6364136223846793005ull + 1442695040888963407ull;
  const u32 tlo = (u32)tag, thi = (u32)(tag >> 32);
  asm volatile("" :: "v"(ct0));
  STAMP(S_part);
  SB();
  TOUCH(0);
  STAMP(S_t0);
  ISSUE(1, rk, ST(1));
  SB();
  lds_barrier();
  {
    const float* src = wave == 0 ? l_y + lane * 2 : l_y + 1024 + (lane & 31) * 2;
    const int st = wave == 0 ? 128 : 64;
    f32x2 t = *(const f32x2*)src;
#pragma unroll
    for (int w = 1; w < 8; ++w) t += *(const f32x2*)(src + w * st);
    BST((u32x4{asu(t[0]), tlo, asu(t[1]), thi}), rw, tid < 64 + FG / 2 ? WS_Q + wg * 2048 + tid * 16 : WS_BYTES, AUX_SC1);
  }
  STAMP(S_qpub);
  SB();
  TOUCH(1);
  STAMP(S_t1);
  ISSUE(2, rk, ST(2));
  const u32x4 w2v = BLD(r2, hi < 7 ? hi * (DM * 2) + lo * 16 : FF * DM * 2, (h * FG + wave * 7) * (DM * 2) + s * 128, 0);
  const u32 b1v = __builtin_amdgcn_raw_buffer_load_b16(rb1, hi * 2, (h * FG + wave * 7) * 2, 0);
  SB();
  u32 qp[4];
  {
    const int sob = WS_Q + (h * 8 + wave) * 2048, voh = 1024 + (lane < FG / 2 ? lane : 0) * 16;
    TOUCH(2);
    STAMP(S_t2);
    u32x4 gq = BLD(rw, vo, sob, AUX_SC1);
    u32x4 gh = BLD(rw, voh, sob, AUX_SC1);
    asm volatile("" ::: "memory");
    ISSUE(3, rk, ST(3));
    SB();
    if (ANY(BAD(gq) | BAD(gh))) {
      do {
        __builtin_amdgcn_s_sleep(1);
        gq = BLD(rw, vo, sob, AUX_SC1);
        gh = BLD(rw, voh, sob, AUX_SC1);
        asm volatile("" : "+v"(gq), "+v"(gh));
      } while (ANY(BAD(gq) | BAD(gh)));
    }
    *(f32x2*)(l_y + 2048 + wave * 128 + lane * 2) = f32x2{asf(gq[0]), asf(gq[2])};
    if (lane < 32) *(f32x2*)(l_h + wave * 64 + lane * 2) = f32x2{asf(gh[0]), asf(gh[2])};
    STAMP(S_gath);
    SB();
    TOUCH(3);
    STAMP(S_t3);
    ISSUE(4, rk, ST(4));
    SB();
    lds_barrier();
    const float* lq = l_y + 2048;
    const f32x4 p0 = *(const f32x4*)(lq + (2 * r) * 128 + c * 8), p1 = *(const f32x4*)(lq + (2 * r) * 128 + c * 8 + 4);
    const f32x4 p2 = *(const f32x4*)(lq + (2 * r + 1) * 128 + c * 8), p3 = *(const f32x4*)(lq + (2 * r + 1) * 128 + c * 8 + 4);
    float qf[8];
#pragma unroll
    for (int k = 0; k < 4; ++k) { qf[k] = xrow_sum(p0[k] + p2[k]); qf[4 + k] = xrow_sum(p1[k] + p3[k]); }
#pragma unroll
    for (int k = 0; k < 4; ++k) qp[k] = pk2(qf[2 * k], qf[2 * k + 1]);
  }
  STAMP(S_qready);
  const bool q0 = lane & 1, q1 = lane & 2;
  float* const ls = l_s + wave * (NS * 16) + r * 4 + (lane & 3);
  float m = -__builtin_inff(), l = 0.f, sc;
  f32x2 acc[4] = {{0, 0}, {0, 0}, {0, 0}, {0, 0}};
#define SCORE(sl) do { \
    float d[U]; \
    _Pragma("unroll") for (int u = 0; u < U; ++u) { \
      d[u] = dot2(tb[sl][u][0], qp[0], 0.f); d[u] = dot2(tb[sl][u][1], qp[1], d[u]); d[u] = dot2(tb[sl][u][2], qp[2], d[u]); d[u] = dot2(tb[sl][u][3], qp[3], d[u]); } \
    const float e0 = (q0 ? d[1] : d[0]) + dpp<XOR1>(q0 ? d[0] : d[1]), e1 = (q0 ? d[3] : d[2]) + dpp<XOR1>(q0 ? d[2] : d[3]); \
    float f = (q1 ? e1 : e0) + dpp<XOR2>(q1 ? e0 : e1); \
    f += dpp<XOR8>(f); f += dpp<ROR4>(f); \
    sc = rbf(rbf(f) * 0.08838834764831845f) * 1.4426950408889634f; \
  } while (0)
#define KTILE(sl, t) do { SCORE(sl); ls[(t) * 16] = sc; m = fmaxf(m, sc); asm volatile("" : "+v"(m) :: "memory"); } while (0)
#define ACCUM(sl) do { \
    const float p = __builtin_amdgcn_exp2f(sc - m); l += p; \
    const float pu[U] = {dpp<0x00>(p), dpp<0x55>(p), dpp<0xAA>(p), dpp<0xFF>(p)}; \
    _Pragma("unroll") for (int u = 0; u < U; ++u) \
      _Pragma("unroll") for (int k = 0; k < 4; ++k) acc[k] = __builtin_elementwise_fma(f32x2{pu[u], pu[u]}, f32x2{lo16(tb[sl][u][k]), hi16(tb[sl][u][k])}, acc[k]); \
    asm volatile("" : "+v"(acc[0]), "+v"(acc[1]), "+v"(acc[2]), "+v"(acc[3]), "+v"(l)); \
  } while (0)
#define VTILE(sl, t) do { sc = ls[(t) * 16]; ACCUM(sl); } while (0)
#define VTILE_RUN(sl) do { \
    float mn = fmaxf(sc, dpp<XOR1>(sc)); mn = fmaxf(mn, dpp<XOR2>(mn)); mn = fmaxf(mn, m); \
    const float alpha = __builtin_amdgcn_exp2f(m - mn); \
    m = mn; l *= alpha; \
    _Pragma("unroll") for (int k = 0; k < 4; ++k) acc[k] *= f32x2{alpha, alpha}; \
    ACCUM(sl); \
  } while (0)
  KTILE(0, 0);
  KTILE(1, 1);
  SB();
  TOUCH(4);
  STAMP(S_t4);
  ISSUE(0, rk, ST(5));
  const u32x4 woa = BLD(ro, lo * 2 * (DM * 2) + hi * 16, (h * DH + wave * 16) * (DM * 2) + s * 128, 0);
  const u32x4 wob = BLD(ro, lo * 2 * (DM * 2) + hi * 16, (h * DH + wave * 16 + 1) * (DM * 2) + s * 128, 0);
  u32 xo = __builtin_amdgcn_raw_buffer_load_b32(rx, lo * 4, h * 32, 0);
  u32 bo = __builtin_amdgcn_raw_buffer_load_b32(mkrsrc(b2, DM * 2), lo * 4, h * 32, 0);
  SB();
  KTILE(2, 2);
  KTILE(3, 3);
  KTILE(4, 4);
  SB();
  TOUCH(0);
  STAMP(S_t5);
  ISSUE(1, rk, ST(6));
  SB();
  {
    float hs = l_h[lo * 64 + wave * 7 + hi];
    hs += dpp<XOR1>(hs); hs += dpp<XOR2>(hs); hs += dpp<XOR7>(hs);
    float hh = rbf(rbf(hs) + lo16(b1v));
    hh = (hh > 0.f && hi < 7) ? hh : 0.f;
    hh = rbf(hh * hh);
    float p[8];
#pragma unroll
    for (int k = 0; k < 4; ++k) { p[2 * k] = hi8_sum(hh * lo16(w2v[k])); p[2 * k + 1] = hi8_sum(hh * hi16(w2v[k])); }
    if (lane < 8) {
      *(f32x4*)(l_b + wave * 64 + lane * 8) = f32x4{p[0], p[1], p[2], p[3]};
      *(f32x4*)(l_b + wave * 64 + lane * 8 + 4) = f32x4{p[4], p[5], p[6], p[7]};
    }
    asm volatile("s_waitcnt lgkmcnt(0)" ::: "memory");
    u32 old = 0;
    if (lane == 0) old = __hip_atomic_fetch_add(l_cnt, 1u, __ATOMIC_RELAXED, __HIP_MEMORY_SCOPE_WORKGROUP);
    old = __builtin_amdgcn_readfirstlane(old);
    asm volatile("" ::: "memory");
    const float* src = l_b + (lane & 31) * 2;
    f32x2 t = *(const f32x2*)src;
#pragma unroll
    for (int w = 1; w < 8; ++w) t += *(const f32x2*)(src + w * 64);
    BST((u32x4{asu(t[0]), tlo, asu(t[1]), thi}), rw, (lane < 32 && old == NW - 1) ? WS_B + wg * 512 + vo : WS_BYTES, AUX_SC1);
  }
  f32x2 wa[4], wb[4];
#pragma unroll
  for (int k = 0; k < 4; ++k) { wa[k] = f32x2{lo16(woa[k]), hi16(woa[k])}; wb[k] = f32x2{lo16(wob[k]), hi16(wob[k])}; }
  asm volatile("" : "+v"(wa[0]), "+v"(wa[1]), "+v"(wa[2]), "+v"(wa[3]), "+v"(wb[0]), "+v"(wb[1]), "+v"(wb[2]), "+v"(wb[3]), "+v"(xo), "+v"(bo));
  SB();
  KTILE(0, 5);
  SB();

  for (int t = NB + 2; t < NS; t += 2) {
#pragma unroll
    for (int ph = 0; ph < 2; ++ph) {
      TOUCH(ph ^ 1);
      ISSUE(ph, rk, ST(t + ph));
      KTILE(ph ^ 1, t + ph - 1);
      SB();
    }
  }
  TOUCH(1);
  STAMP(S_kend);
  ISSUE(0, rv, ST(0));
  KTILE(1, NS - 1);
  m = fmaxf(m, dpp<XOR1>(m)); m = fmaxf(m, dpp<XOR2>(m)); m = xrow_max(m);
  SB();
  for (int t = 0; t < NS - 3; t += 2) {
#pragma unroll
    for (int ph = 0; ph < 2; ++ph) {
      TOUCH(ph);
      ISSUE(ph ^ 1, rv, ST(t + ph + 1));
      VTILE(ph, t + ph);
      SB();
    }
  }
  STAMP(S_statend);
  {
    int cvo = lane == 0 ? WS_CT + h * 256 : WS_BYTES;
    asm volatile("" : "+v"(cvo));
    int tk = 0;
#define DRAW() do { tk = __builtin_amdgcn_raw_ptr_buffer_atomic_add_i32(1, rw, cvo, 0, 0); asm volatile("" ::: "memory"); } while (0)
    TOUCH(0);
    ISSUE(1, rv, ST(NS - 2));
    DRAW();
    VTILE(0, NS - 3);
    SB();
    TOUCH(1);
    ISSUE(0, rv, ST(NS - 1));
    VTILE(1, NS - 2);
    sc = ls[(NS - 1) * 16];
    SB();
    while (true) {
      const u32 g = __builtin_amdgcn_readfirstlane(tk);
      if (g >= (u32)NGRP) break;
      const int o = (g & 7) * CHUNK + NS * (NW * TILE) + (g >> 3) * TILE;
      TOUCH(0);
      ISSUE(1, rk, o);
      DRAW();
      VTILE_RUN(0);
      SB();
      TOUCH(1);
      ISSUE(0, rv, o);
      SCORE(1);
      asm volatile("" : "+v"(sc));
      SB();
    }
  }
  const bool fin = s == (h & 7);
  const rsrc_t rwb = mkrsrc(ws, fin && wave == 1 ? WS_BYTES : 0);
  const int voy = hi * 4096 + lo * 16, soy = (h >> 2) * 512 + (h & 3) * 128;
  u32x4 pb[4];
#pragma unroll
  for (int j = 0; j < 4; ++j) pb[j] = BLD(rwb, voy, WS_B + soy + j * 32768, AUX_SC1);
  asm volatile("" ::: "memory");
  SB();
  VTILE_RUN(0);
  l += dpp<XOR1>(l); l += dpp<XOR2>(l);
  STAMP(S_loopend);

  {
    const float mw = xrow_max(m);
    const float f = __builtin_amdgcn_exp2f(m - mw);
    l = xrow_sum(l * f);
    float a[8];
#pragma unroll
    for (int k = 0; k < 4; ++k) { a[2 * k] = xrow_sum(acc[k][0] * f); a[2 * k + 1] = xrow_sum(acc[k][1] * f); }
    if (r == 0) {
      *(f32x4*)(l_acc + wave * 132 + c * 8) = f32x4{a[0], a[1], a[2], a[3]};
      *(f32x4*)(l_acc + wave * 132 + c * 8 + 4) = f32x4{a[4], a[5], a[6], a[7]};
      if (c == 0) *(f32x2*)(l_acc + wave * 132 + 128) = f32x2{mw, l};
    }
  }
  lds_barrier();
  STAMP(S_bar2);
  if (wave == 0) {
    f32x2 ml[8], ov[8];
#pragma unroll
    for (int w = 0; w < 8; ++w) { ml[w] = *(const f32x2*)(l_acc + w * 132 + 128); ov[w] = *(const f32x2*)(l_acc + w * 132 + lane * 2); }
    float Mg = ml[0][0], Lg = 0.f;
#pragma unroll
    for (int w = 1; w < 8; ++w) Mg = fmaxf(Mg, ml[w][0]);
    f32x2 o = {0, 0};
#pragma unroll
    for (int w = 0; w < 8; ++w) {
      const float f = __builtin_amdgcn_exp2f(ml[w][0] - Mg);
      Lg = fmaf(f, ml[w][1], Lg);
      o = __builtin_elementwise_fma(f32x2{f, f}, ov[w], o);
    }
    BST((u32x4{asu(o[0]), tlo, asu(o[1]), thi}), rw, WS_O + wg * 2048 + vo, AUX_SC1);
    BST((u32x4{asu(Mg), tlo, asu(Lg), thi}), rw, lane == 0 ? WS_O + wg * 2048 + 1024 : WS_BYTES, AUX_SC1);
    STAMP(S_opub);
  }
  {
    const int sob = WS_O + h * 8 * 2048;
    u32x4 go = BLD(rw, hi * 2048 + lo * 16, sob + wave * 128, AUX_SC1);
    u32x4 gm = BLD(rw, hi * 2048, sob + 1024, AUX_SC1);
    asm volatile("" ::: "memory");
    SB();
    while (ANY(BAD(go) | BAD(gm))) {
      __builtin_amdgcn_s_sleep(CFG_SLEEP);
      go = BLD(rw, hi * 2048 + lo * 16, sob + wave * 128, AUX_SC1);
      gm = BLD(rw, hi * 2048, sob + 1024, AUX_SC1);
      asm volatile("" ::: "memory");
    }
    STAMP(S_opoll);
    const float mj = asf(gm[0]);
    const float M = xrow_max(fmaxf(mj, dpp<XOR8>(mj)));
    const float e = __builtin_amdgcn_exp2f(mj - M);
    const float L = hi8_sum(e * asf(gm[2]));
    const float rl = __builtin_amdgcn_rcpf(L);
    const float oa = hi8_sum(e * asf(go[0])) * rl, ob = hi8_sum(e * asf(go[2])) * rl;
    float y[8];
#pragma unroll
    for (int k = 0; k < 4; ++k) {
      const f32x2 t = __builtin_elementwise_fma(f32x2{ob, ob}, wb[k], f32x2{oa, oa} * wa[k]);
      y[2 * k] = t[0]; y[2 * k + 1] = t[1];
    }
#pragma unroll
    for (int k = 0; k < 8; ++k) { y[k] += dpp<XOR1>(y[k]); y[k] += dpp<XOR2>(y[k]); y[k] += dpp<XOR7>(y[k]); }
    if (lo == 0) {
      *(f32x4*)(l_y + wave * 64 + hi * 8) = f32x4{y[0], y[1], y[2], y[3]};
      *(f32x4*)(l_y + wave * 64 + hi * 8 + 4) = f32x4{y[4], y[5], y[6], y[7]};
    }
  }
  lds_barrier();
  STAMP(S_bar3);
  if (wave == 0) {
    const float* src = l_y + (lane >> 5) * 256 + (lane & 31) * 2;
    const f32x2 t = (*(const f32x2*)src + *(const f32x2*)(src + 64)) + (*(const f32x2*)(src + 128) + *(const f32x2*)(src + 192));
    const auto u0 = __builtin_amdgcn_permlane32_swap(asu(t[0]), asu(t[0]), false, false);
    const auto u1 = __builtin_amdgcn_permlane32_swap(asu(t[1]), asu(t[1]), false, false);
    BST((u32x4{asu(asf(u0[0]) + asf(u0[1])), tlo, asu(asf(u1[0]) + asf(u1[1])), thi}), rw, lane < 32 ? WS_Y + wg * 512 + vo : WS_BYTES, AUX_SC1);
    STAMP(S_ypub);
  } else if (wave == 1 && fin) {
    u32x4 py[4];
#pragma unroll
    for (int j = 0; j < 4; ++j) py[j] = BLD(rw, voy, WS_Y + soy + j * 32768, AUX_SC1);
    asm volatile("" ::: "memory");
    SB();
    if (ANY(BAD(pb[0]) | BAD(pb[1]) | BAD(pb[2]) | BAD(pb[3]))) {
      do {
        __builtin_amdgcn_s_sleep(1);
#pragma unroll
        for (int j = 0; j < 4; ++j) pb[j] = BLD(rw, voy, WS_B + soy + j * 32768, AUX_SC1);
        asm volatile("" : "+v"(pb[0]), "+v"(pb[1]), "+v"(pb[2]), "+v"(pb[3]) :: "memory");
      } while (ANY(BAD(pb[0]) | BAD(pb[1]) | BAD(pb[2]) | BAD(pb[3])));
    }
    const float c0 = hi8_sum((asf(pb[0][0]) + asf(pb[1][0])) + (asf(pb[2][0]) + asf(pb[3][0])));
    const float c1 = hi8_sum((asf(pb[0][2]) + asf(pb[1][2])) + (asf(pb[2][2]) + asf(pb[3][2])));
    float B0 = rbf(rbf(c0) + lo16(bo)), B1 = rbf(rbf(c1) + hi16(bo));
    asm volatile("" : "+v"(B0), "+v"(B1));
    SB();
    while (ANY(BAD(py[0]) | BAD(py[1]) | BAD(py[2]) | BAD(py[3]))) {
      __builtin_amdgcn_s_sleep(CFG_SLEEP);
#pragma unroll
      for (int j = 0; j < 4; ++j) py[j] = BLD(rw, voy, WS_Y + soy + j * 32768, AUX_SC1);
      asm volatile("" ::: "memory");
    }
    STAMP(S_ypoll);
    const float A0 = hi8_sum((asf(py[0][0]) + asf(py[1][0])) + (asf(py[2][0]) + asf(py[3][0])));
    const float A1 = hi8_sum((asf(py[0][2]) + asf(py[1][2])) + (asf(py[2][2]) + asf(py[3][2])));
    const u32 res = pk2(rbf(lo16(xo) + rbf(A0)) + B0, rbf(hi16(xo) + rbf(A1)) + B1);
    if (lane < 8) ((u32*)out)[h * 8 + lane] = res;
    if (h == 0 && lane == 8) __hip_atomic_store((u64*)(ws + WS_GEN), tag, __ATOMIC_RELAXED, __HIP_MEMORY_SCOPE_AGENT);
    STAMP(S_final);
  }
  FLUSH();
}

extern "C" void kernel_launch(void* const* d_in, const int* in_sizes, int n_in, void* d_out, int out_size, void* d_ws,
                              size_t ws_size, hipStream_t stream) {
  fused_kernel<<<NWG, NTHR, 0, stream>>>((const u16*)d_in[0], (const u16*)d_in[1], (const u16*)d_in[2], (const u16*)d_in[3],
                                         (const u16*)d_in[4], (const u16*)d_in[6], (const u16*)d_in[7], (const u16*)d_in[8],
                                         (const u16*)d_in[9], (u16*)d_out, (unsigned char*)d_ws);
}
```

```cpp
#include <hip/hip_runtime.h>
#include <stdint.h>

#define CFG_NS 60
#define CFG_NB 6
#define CFG_D 2
#define CFG_GS0 30
#define CFG_GS1 10
#define CFG_TAU 438
#define CFG_TOFF 100
#define CFG_ROFF 50
#define CFG_SP 0
#define CFG_SLEEP 1
#define CFG_FDELAY 0

typedef unsigned int u32;
typedef unsigned long long u64;
typedef unsigned short u16;
typedef u32 u32x4 __attribute__((ext_vector_type(4)));
typedef float f32x4 __attribute__((ext_vector_type(4)));
typedef float f32x2 __attribute__((ext_vector_type(2)));
typedef __bf16 bf16x2 __attribute__((ext_vector_type(2)));
typedef __amdgpu_buffer_rsrc_t rsrc_t;

constexpr int DM = 512, NH = 32, SK = 65536, DH = 128, FF = 1792;
constexpr int NWG = 256, NW = 8, NSV = 3, NTHR = (NW + NSV) * 64;
constexpr int CHUNK = SK / 8 * DH * 2;
constexpr int WS_GEN = 0;
constexpr int WS_CT = 8192;
constexpr int WS_B = 16384;
constexpr int WS_Q = WS_B + NWG * 512;
constexpr int WS_O = WS_Q + NWG * 2048;
constexpr int WS_Y = WS_O + NWG * 2048;
constexpr int WS_BYTES = WS_Y + NWG * 512;
constexpr int AUX_NT = 2, AUX_W = 2, AUX_SC1 = 16, AUX_ST = 16;

__device__ __forceinline__ rsrc_t mkrsrc(const void* p, int bytes) {
  return __builtin_amdgcn_make_buffer_rsrc((void*)p, 0, bytes, 0x00020000);
}
__device__ __forceinline__ float asf(u32 x) { return __builtin_bit_cast(float, x); }
__device__ __forceinline__ u32 asu(float x) { return __builtin_bit_cast(u32, x); }
__device__ __forceinline__ float lo16(u32 x) { return asf(x << 16); }
__device__ __forceinline__ float hi16(u32 x) { return asf(x & 0xffff0000u); }
__device__ __forceinline__ float rbf(float x) { __bf16 b = (__bf16)x; return (float)b; }
__device__ __forceinline__ u32 pk2(float a, float b) { return __builtin_bit_cast(u32, bf16x2{(__bf16)a, (__bf16)b}); }
__device__ __forceinline__ float dot2(u32 a, u32 b, float acc) {
  return __builtin_amdgcn_fdot2_f32_bf16(__builtin_bit_cast(bf16x2, a), __builtin_bit_cast(bf16x2, b), acc, false);
}
template <int CTRL> __device__ __forceinline__ float dpp(float x) {
  return __builtin_bit_cast(float, __builtin_amdgcn_mov_dpp(__builtin_bit_cast(int, x), CTRL, 0xf, 0xf, true));
}
constexpr int XOR1 = 0xB1, XOR2 = 0x4E, XOR7 = 0x141, XOR8 = 0x128, ROR4 = 0x124;
__device__ __forceinline__ float row16_sum(float x) {
  x += dpp<XOR8>(x); x += dpp<XOR7>(x); x += dpp<XOR2>(x); x += dpp<XOR1>(x); return x;
}
__device__ __forceinline__ float xrow_sum(float x) {
  auto s = __builtin_amdgcn_permlane16_swap(asu(x), asu(x), false, false);
  x = asf(s[0]) + asf(s[1]);
  auto t = __builtin_amdgcn_permlane32_swap(asu(x), asu(x), false, false);
  return asf(t[0]) + asf(t[1]);
}
__device__ __forceinline__ float xrow_max(float x) {
  auto s = __builtin_amdgcn_permlane16_swap(asu(x), asu(x), false, false);
  x = fmaxf(asf(s[0]), asf(s[1]));
  auto t = __builtin_amdgcn_permlane32_swap(asu(x), asu(x), false, false);
  return fmaxf(asf(t[0]), asf(t[1]));
}
__device__ __forceinline__ float hi8_sum(float x) { x += dpp<XOR8>(x); return xrow_sum(x); }
__device__ __forceinline__ float wave_sum(float x) { return xrow_sum(row16_sum(x)); }
__device__ __forceinline__ void lds_barrier() {
  asm volatile("s_waitcnt lgkmcnt(0)" ::: "memory");
  __builtin_amdgcn_s_barrier();
  asm volatile("" ::: "memory");
}
#define BLD(r, vo, so, aux) __builtin_amdgcn_raw_buffer_load_b128(r, vo, so, aux)
#define BST(v, r, vo, aux) __builtin_amdgcn_raw_buffer_store_b128(v, r, vo, 0, aux)
#define BAD(v) (((v)[1] ^ tlo) | ((v)[3] ^ thi))
#define ANY(x) (__builtin_amdgcn_ballot_w64((x) != 0) != 0)
#define SB() __builtin_amdgcn_sched_barrier(0)

#define STAMP(i) do { } while (0)
#define FLUSH(w) do { } while (0)

__device__ __forceinline__ f32x2 hi8_fold(const f32x2* a) {
  float b[4];
#pragma unroll
  for (int i = 0; i < 4; ++i) {
    const auto t = __builtin_amdgcn_permlane32_swap(asu(a[i >> 1][i & 1]), asu(a[2 + (i >> 1)][i & 1]), false, false);
    b[i] = asf(t[0]) + asf(t[1]);
  }
  f32x2 o;
#pragma unroll
  for (int i = 0; i < 2; ++i) {
    const auto t = __builtin_amdgcn_permlane16_swap(asu(b[i]), asu(b[2 + i]), false, false);
    const float v = asf(t[0]) + asf(t[1]);
    o[i] = v + dpp<XOR8>(v);
  }
  return o;
}
__device__ __forceinline__ float half_sum(float x) {
  const auto t = __builtin_amdgcn_permlane32_swap(asu(x), asu(x), false, false);
  return asf(t[0]) + asf(t[1]);
}

__global__ void __launch_bounds__(NTHR)
fused_kernel(const u16* x, const u16* K, const u16* V, const u16* wq, const u16* wo, const u16* w1, const u16* b1,
             const u16* w2, const u16* b2, u16* out, unsigned char* ws
) {
  constexpr int U = 4, NB = CFG_NB, NS = CFG_NS, D = CFG_D, FG = FF / NH;
  constexpr int TILE = U * 1024, NT = CHUNK / (TILE * NW), NGRP = (NT - NS) * NW;
  static_assert(NS % D == 0 && NB % D == 0 && NS >= NB + 2 * D && NS <= NT, "");
  __shared__ __attribute__((aligned(16))) float lds[24 * 1024];
  float* const l_y = lds;
  float* const l_acc = lds + 512;
  float* const l_h = l_acc + 8 * 132;
  u32* const l_q = (u32*)(l_h + 64);
  float* const l_s = l_h + 64 + 128;

  const int wg = blockIdx.x, tid = threadIdx.x, lane = tid & 63;
  const int wv = __builtin_amdgcn_readfirstlane(tid >> 6), wave = (wv - NSV) & 7;
  const int r = lane >> 4, c = lane & 15, hi = lane >> 3, lo = lane & 7;
  const int h = wg >> 3, s = wg & 7;
  const int vo = lane * 16;
  const rsrc_t rw = mkrsrc(ws, WS_BYTES);

  STAMP(S_start);
  asm volatile("" :: "s"(K), "s"(b2), "s"(out), "s"(ws));
  const rsrc_t rk = mkrsrc((const char*)K + (size_t)h * (8 * CHUNK), wv < NSV ? 0 : 8 * CHUNK);
  const int sb = s * CHUNK + wave * TILE;
  u32x4 tb[NB][U];
#define ISSUE(sl, rs_, so_) do { _Pragma("unroll") for (int u = 0; u < U; ++u) tb[sl][u] = BLD(rs_, vo + u * 1024, so_, AUX_NT); asm volatile("" ::: "memory"); } while (0)
#define TOUCH(sl) do { _Pragma("unroll") for (int u = 0; u < U; ++u) asm volatile("" : "+v"(tb[sl][u]) :: "memory"); } while (0)
#define ST(t) (sb + (t) * (NW * TILE))
  ISSUE(0, rk, ST(0));
  const u32 tbase = (u32)__builtin_amdgcn_s_memrealtime();
  if (tid < 2) l_q[66 + tid] = 0;
  lds_barrier();
  SB();

  if (__builtin_expect(wv < NSV, 1)) {
    const rsrc_t rx = mkrsrc(x, DM * 2);
    const bool ffn = wv == 2;
    const rsrc_t ra = ffn ? mkrsrc(w1, DM * FF * 2) : mkrsrc(wq, DM * NH * DH * 2);
    const int pitch = ffn ? FF * 2 : NH * DH * 2;
    const int voa = (ffn && lo == 7) ? DM * FF * 2 : hi * 8 * pitch + lo * 16;
    const int soa = s * 64 * pitch + (ffn ? h * (FG * 2) : h * 256 + wv * 128);
    if (ffn) for (int i = 0; i < CFG_FDELAY; ++i) __builtin_amdgcn_s_sleep(32);
    asm volatile("" ::: "memory");
    const u32x4 xs = BLD(rx, vo, 0, 0);
    const u32x4 xv = BLD(rx, hi * 16, s * 128, 0);
    u32x4 wa[8];
#pragma unroll
    for (int j = 0; j < 8; ++j) wa[j] = BLD(ra, voa, soa + j * pitch, AUX_W);
    const auto gen2 = __builtin_amdgcn_raw_buffer_load_b64(rw, 0, WS_GEN, AUX_SC1);
    u32 ct0 = 0;
    if (s == 0 && tid < 8) ct0 = __hip_atomic_exchange((u32*)(ws + WS_CT + h * 256) + tid, 0u, __ATOMIC_RELAXED, __HIP_MEMORY_SCOPE_AGENT);
    SB();
    const u32 b1v = __builtin_amdgcn_raw_buffer_load_b32(mkrsrc(b1, ffn ? FF * 2 : 0), (lane & 31) < FG / 2 ? (lane & 31) * 4 : FF * 2, h * (FG * 2), 0);
    const u32 xo = __builtin_amdgcn_raw_buffer_load_b32(rx, lo * 4, h * 32, 0);
    const u32 bo = __builtin_amdgcn_raw_buffer_load_b32(mkrsrc(b2, DM * 2), lo * 4, h * 32, 0);
    asm volatile("" ::: "memory");
    SB();
    float xn[8];
    {
      float e[8];
#pragma unroll
      for (int k = 0; k < 4; ++k) { e[2 * k] = lo16(xs[k]); e[2 * k + 1] = hi16(xs[k]); }
      float sum = ((e[0] + e[1]) + (e[2] + e[3])) + ((e[4] + e[5]) + (e[6] + e[7]));
      const float mean = wave_sum(sum) * (1.0f / DM);
      float sq = 0.f;
#pragma unroll
      for (int k = 0; k < 8; ++k) { const float d = e[k] - mean; sq = fmaf(d, d, sq); }
      const float var = wave_sum(sq) * (1.0f / DM);
      const float mean_b = rbf(mean);
      const float sd = rbf(__builtin_amdgcn_sqrtf(rbf(rbf(var) + 1e-5f)));
      const float rstd = __builtin_amdgcn_rcpf(sd);
#pragma unroll
      for (int k = 0; k < 4; ++k) { xn[2 * k] = rbf(rbf(lo16(xv[k]) - mean_b) * rstd); xn[2 * k + 1] = rbf(rbf(hi16(xv[k]) - mean_b) * rstd); }
    }
    asm volatile("" : "+v"(xn[0]), "+v"(xn[1]), "+v"(xn[2]), "+v"(xn[3]), "+v"(xn[4]), "+v"(xn[5]), "+v"(xn[6]), "+v"(xn[7]));
    SB();
    STAMP(S_ln);
    f32x2 a[4] = {{0, 0}, {0, 0}, {0, 0}, {0, 0}};
#pragma unroll
    for (int j = 0; j < 8; ++j)
#pragma unroll
      for (int k = 0; k < 4; ++k) a[k] = __builtin_elementwise_fma(f32x2{xn[j], xn[j]}, f32x2{lo16(wa[j][k]), hi16(wa[j][k])}, a[k]);
    const f32x2 part = hi8_fold(a);
    const u64 gen = ((u64)(u32)__builtin_amdgcn_readfirstlane(gen2[1]) << 32) | (u32)__builtin_amdgcn_readfirstlane(gen2[0]);
    const u64 tag = gen * 6364136223846793005ull + 1442695040888963407ull;
    const u32 tlo = (u32)tag, thi = (u32)(tag >> 32);
    asm volatile("" :: "v"(ct0));
    STAMP(S_part);
    const int vop = (lo * 4 + r) * 16;
    BST((u32x4{asu(part[0]), tlo, asu(part[1]), thi}), rw, (lane & 8) ? WS_BYTES : WS_Q + wg * 2048 + wv * 512 + vop, AUX_ST);
    STAMP(S_qpub);
    f32x2 g;
    {
      const int vog = (lane >> 5) * 2048 + (lane & 31) * 16, sog = WS_Q + h * 8 * 2048 + wv * 512;
      u32x4 gq[4];
      if (ffn) __builtin_amdgcn_s_sleep(100); else if (CFG_GS0) __builtin_amdgcn_s_sleep(CFG_GS0);
      while (true) {
#pragma unroll
        for (int j = 0; j < 4; ++j) gq[j] = BLD(rw, vog, sog + j * 4096, AUX_SC1);
        asm volatile("" : "+v"(gq[0]), "+v"(gq[1]), "+v"(gq[2]), "+v"(gq[3]) :: "memory");
        if (!ANY(BAD(gq[0]) | BAD(gq[1]) | BAD(gq[2]) | BAD(gq[3]))) break;
        if (CFG_GS1) __builtin_amdgcn_s_sleep(CFG_GS1);
      }
      g[0] = half_sum((asf(gq[0][0]) + asf(gq[1][0])) + (asf(gq[2][0]) + asf(gq[3][0])));
      g[1] = half_sum((asf(gq[0][2]) + asf(gq[1][2])) + (asf(gq[2][2]) + asf(gq[3][2])));
    }
    STAMP(S_gath);
    if (!ffn) {
      if (lane < 32) l_q[wv * 32 + lane] = pk2(g[0], g[1]);
      if (lane == 0) { l_q[64] = tlo; l_q[65] = thi; }
      asm volatile("s_waitcnt lgkmcnt(0)" ::: "memory");
      if (lane == 0) __hip_atomic_fetch_add(l_q + 66, 1u, __ATOMIC_RELAXED, __HIP_MEMORY_SCOPE_WORKGROUP);
      STAMP(S_qready);
      lds_barrier();
      STAMP(S_bar2);
      if (wv == 0) {
        f32x2 ml[8], ov[8];
#pragma unroll
        for (int w = 0; w < 8; ++w) { ml[w] = *(const f32x2*)(l_acc + w * 132 + 128); ov[w] = *(const f32x2*)(l_acc + w * 132 + lane * 2); }
        float Mg = ml[0][0], Lg = 0.f;
#pragma unroll
        for (int w = 1; w < 8; ++w) Mg = fmaxf(Mg, ml[w][0]);
        f32x2 o = {0, 0};
#pragma unroll
        for (int w = 0; w < 8; ++w) {
          const float f = __builtin_amdgcn_exp2f(ml[w][0] - Mg);
          Lg = fmaf(f, ml[w][1], Lg);
          o = __builtin_elementwise_fma(f32x2{f, f}, ov[w], o);
        }
        BST((u32x4{asu(o[0]), tlo, asu(o[1]), thi}), rw, WS_O + wg * 2048 + vo, AUX_ST);
        BST((u32x4{asu(Mg), tlo, asu(Lg), thi}), rw, lane == 0 ? WS_O + wg * 2048 + 1024 : WS_BYTES, AUX_ST);
        STAMP(S_opub);
      }
      lds_barrier();
      if (wv == 0) {
        const float* src = l_y + (lane >> 5) * 256 + (lane & 31) * 2;
        const f32x2 t = (*(const f32x2*)src + *(const f32x2*)(src + 64)) + (*(const f32x2*)(src + 128) + *(const f32x2*)(src + 192));
        BST((u32x4{asu(half_sum(t[0])), tlo, asu(half_sum(t[1])), thi}), rw, lane < 32 ? WS_Y + wg * 512 + vo : WS_BYTES, AUX_ST);
        STAMP(S_ypub);
      }
    } else {
      {
        float h0 = rbf(rbf(g[0]) + lo16(b1v)), h1 = rbf(rbf(g[1]) + hi16(b1v));
        h0 = h0 > 0.f ? h0 : 0.f; h1 = h1 > 0.f ? h1 : 0.f;
        if (lane < 32) *(f32x2*)(l_h + lane * 2) = f32x2{rbf(h0 * h0), rbf(h1 * h1)};
        asm volatile("s_waitcnt lgkmcnt(0)" ::: "memory");
        while (__hip_atomic_load(l_q + 67, __ATOMIC_RELAXED, __HIP_MEMORY_SCOPE_WORKGROUP) == 0) __builtin_amdgcn_s_sleep(4);
        asm volatile("" ::: "memory");
        const rsrc_t r2 = mkrsrc(w2, FF * DM * 2);
        u32x4 wb[7];
#pragma unroll
        for (int j = 0; j < 7; ++j) wb[j] = BLD(r2, hi * (DM * 2) + lo * 16, (h * FG + j * 8) * (DM * 2) + s * 128, AUX_W);
        asm volatile("" ::: "memory");
        lds_barrier();
        f32x2 y[4] = {{0, 0}, {0, 0}, {0, 0}, {0, 0}};
#pragma unroll
        for (int j = 0; j < 7; ++j) {
          const float hh = l_h[j * 8 + hi];
#pragma unroll
          for (int k = 0; k < 4; ++k) y[k] = __builtin_elementwise_fma(f32x2{hh, hh}, f32x2{lo16(wb[j][k]), hi16(wb[j][k])}, y[k]);
        }
        const f32x2 t = hi8_fold(y);
        BST((u32x4{asu(t[0]), tlo, asu(t[1]), thi}), rw, (lane & 8) ? WS_BYTES : WS_B + wg * 512 + vop, AUX_ST);
        STAMP(S_bpub);
      }
      const bool fin = s == (h & 7);
      const int voy = hi * 4096 + lo * 16, soy = (h >> 2) * 512 + (h & 3) * 128;
      lds_barrier();
      if (fin) {
        u32x4 pb[4], pc[4];
        while (true) {
#pragma unroll
          for (int j = 0; j < 4; ++j) pc[j] = BLD(rw, voy, WS_B + soy + j * 32768, AUX_SC1);
#pragma unroll
          for (int j = 0; j < 4; ++j) pb[j] = BLD(rw, voy, WS_Y + soy + j * 32768, AUX_SC1);
          asm volatile("" : "+v"(pc[0]), "+v"(pc[1]), "+v"(pc[2]), "+v"(pc[3]) :: "memory");
          if (!ANY(BAD(pc[0]) | BAD(pc[1]) | BAD(pc[2]) | BAD(pc[3]))) break;
          __builtin_amdgcn_s_sleep(CFG_SLEEP);
        }
        const float c0 = hi8_sum((asf(pc[0][0]) + asf(pc[1][0])) + (asf(pc[2][0]) + asf(pc[3][0])));
        const float c1 = hi8_sum((asf(pc[0][2]) + asf(pc[1][2])) + (asf(pc[2][2]) + asf(pc[3][2])));
        float B0 = rbf(rbf(c0) + lo16(bo)), B1 = rbf(rbf(c1) + hi16(bo));
        asm volatile("" : "+v"(B0), "+v"(B1), "+v"(pb[0]), "+v"(pb[1]), "+v"(pb[2]), "+v"(pb[3]));
        while (ANY(BAD(pb[0]) | BAD(pb[1]) | BAD(pb[2]) | BAD(pb[3]))) {
#pragma unroll
          for (int j = 0; j < 4; ++j) pb[j] = BLD(rw, voy, WS_Y + soy + j * 32768, AUX_SC1);
          asm volatile("" : "+v"(pb[0]), "+v"(pb[1]), "+v"(pb[2]), "+v"(pb[3]) :: "memory");
        }
        STAMP(S_ypoll);
        const float A0 = hi8_sum((asf(pb[0][0]) + asf(pb[1][0])) + (asf(pb[2][0]) + asf(pb[3][0])));
        const float A1 = hi8_sum((asf(pb[0][2]) + asf(pb[1][2])) + (asf(pb[2][2]) + asf(pb[3][2])));
        const u32 res = pk2(rbf(lo16(xo) + rbf(A0)) + B0, rbf(hi16(xo) + rbf(A1)) + B1);
        if (lane < 8) ((u32*)out)[h * 8 + lane] = res;
        if (h == 0 && lane == 8) __hip_atomic_store((u64*)(ws + WS_GEN), tag, __ATOMIC_RELAXED, __HIP_MEMORY_SCOPE_AGENT);
        STAMP(S_final);
      }
    }
    FLUSH(wv);
    return;
  }

  u32 due = (tbase + CFG_ROFF) * 4 + CFG_TAU + ((wave + h) & 7) * CFG_TAU / 8;
#define PACE() do { while ((int)((u32)__builtin_amdgcn_s_memrealtime() * 4 - due) < 0) __builtin_amdgcn_s_sleep(1); due += CFG_TAU; } while (0)
  const rsrc_t rv = mkrsrc((const char*)V + (size_t)h * (8 * CHUNK), 8 * CHUNK);
  asm volatile("" ::: "memory");
  SB();
  STAMP(S_issued);
#pragma unroll
  for (int t = 1; t < NB; ++t) {
    PACE();
    if (t <= 5) STAMP(S_t0 + t - 1);
    ISSUE(t, rk, ST(t));
    SB();
  }
  due += (CFG_TOFF - CFG_ROFF) * 4;
  while (__hip_atomic_load(l_q + 66, __ATOMIC_RELAXED, __HIP_MEMORY_SCOPE_WORKGROUP) != 2) __builtin_amdgcn_s_sleep(1);
  asm volatile("" ::: "memory");
  STAMP(S_flag);
  const u32x4 qp = *(const u32x4*)(l_q + c * 4);
  const u32 tlo = __builtin_amdgcn_readfirstlane(l_q[64]), thi = __builtin_amdgcn_readfirstlane(l_q[65]);
  const bool q0 = lane & 1, q1 = lane & 2;
  float* const ls = l_s + wave * (NS * 16) + r * 4 + (lane & 3);
  float m = -__builtin_inff(), l = 0.f, sc;
  f32x2 acc[4] = {{0, 0}, {0, 0}, {0, 0}, {0, 0}};
#define SCORE(sl) do { \
    float d[U]; \
    _Pragma("unroll") for (int u = 0; u < U; ++u) { \
      d[u] = dot2(tb[sl][u][0], qp[0], 0.f); d[u] = dot2(tb[sl][u][1], qp[1], d[u]); d[u] = dot2(tb[sl][u][2], qp[2], d[u]); d[u] = dot2(tb[sl][u][3], qp[3], d[u]); } \
    const float e0 = (q0 ? d[1] : d[0]) + dpp<XOR1>(q0 ? d[0] : d[1]), e1 = (q0 ? d[3] : d[2]) + dpp<XOR1>(q0 ? d[2] : d[3]); \
    float f = (q1 ? e1 : e0) + dpp<XOR2>(q1 ? e0 : e1); \
    f += dpp<XOR8>(f); f += dpp<ROR4>(f); \
    sc = rbf(rbf(f) * 0.08838834764831845f) * 1.4426950408889634f; \
  } while (0)
#define KTILE(sl, t) do { SCORE(sl); ls[(t) * 16] = sc; m = fmaxf(m, sc); asm volatile("" : "+v"(m) :: "memory"); } while (0)
#define ACCUM(sl) do { \
    const float p = __builtin_amdgcn_exp2f(sc - m); l += p; \
    const float pu[U] = {dpp<0x00>(p), dpp<0x55>(p), dpp<0xAA>(p), dpp<0xFF>(p)}; \
    _Pragma("unroll") for (int u = 0; u < U; ++u) \
      _Pragma("unroll") for (int k = 0; k < 4; ++k) acc[k] = __builtin_elementwise_fma(f32x2{pu[u], pu[u]}, f32x2{lo16(tb[sl][u][k]), hi16(tb[sl][u][k])}, acc[k]); \
    asm volatile("" : "+v"(acc[0]), "+v"(acc[1]), "+v"(acc[2]), "+v"(acc[3]), "+v"(l)); \
  } while (0)
#define VTILE(sl, t) do { sc = ls[(t) * 16]; ACCUM(sl); } while (0)
#define VTILE_RUN(sl) do { \
    float mn = fmaxf(sc, dpp<XOR1>(sc)); mn = fmaxf(mn, dpp<XOR2>(mn)); mn = fmaxf(mn, m); \
    const float alpha = __builtin_amdgcn_exp2f(m - mn); \
    m = mn; l *= alpha; \
    _Pragma("unroll") for (int k = 0; k < 4; ++k) acc[k] *= f32x2{alpha, alpha}; \
    ACCUM(sl); \
  } while (0)
#pragma unroll
  for (int t = 0; t < NB - D; ++t) KTILE(t, t);
#pragma unroll
  for (int ph = 0; ph < D; ++ph) {
    TOUCH(NB - D + ph);
    KTILE(NB - D + ph, NB - D + ph);
    SB();
    PACE();
    ISSUE(ph, rk, ST(NB + ph));
  }
  STAMP(S_t5);
  for (int t = NB; t < NS - D; t += D) {
#pragma unroll
    for (int ph = 0; ph < D; ++ph) {
      TOUCH(ph);
      KTILE(ph, t + ph);
      SB();
      PACE();
      ISSUE(ph, rk, ST(t + ph + D));
    }
  }
#pragma unroll
  for (int ph = 0; ph < D; ++ph) {
    TOUCH(ph);
    KTILE(ph, NS - D + ph);
    SB();
    PACE();
    ISSUE(ph, rv, ST(ph));
  }
  STAMP(S_kend);
  m = fmaxf(m, dpp<XOR1>(m)); m = fmaxf(m, dpp<XOR2>(m)); m = xrow_max(m);
  for (int t = 0; t < NS - 2 * D; t += D) {
#pragma unroll
    for (int ph = 0; ph < D; ++ph) {
      TOUCH(ph);
      VTILE(ph, t + ph);
      SB();
      PACE();
      ISSUE(ph, rv, ST(t + ph + D));
    }
  }
  STAMP(S_statend);
  constexpr int KS = D - 2, VS = D - 1;
  {
    int cvo = lane < 8 ? WS_CT + h * 256 + lane * 4 : WS_BYTES;
    int vic = s;
    asm volatile("" : "+v"(cvo));
    int tk = 0;
#define DRAW() do { tk = __builtin_amdgcn_raw_ptr_buffer_atomic_add_i32((int)(lane == vic), rw, cvo, 0, 0); asm volatile("" ::: "memory"); } while (0)
#pragma unroll
    for (int ph = 0; ph < D; ++ph) {
      TOUCH(ph);
      VTILE(ph, NS - 2 * D + ph);
      SB();
      PACE();
      ISSUE(ph, rv, ST(NS - D + ph));
      if (ph == D - 2) DRAW();
    }
#pragma unroll
    for (int ph = 0; ph < D - 1; ++ph) {
      TOUCH(ph);
      VTILE(ph, NS - D + ph);
    }
    sc = ls[(NS - 1) * 16];
    SB();
    while (true) {
      u32 g = __builtin_amdgcn_readlane(tk, vic);
      while (g >= (u32)NGRP) {
        const u32 m8 = (u32)__builtin_amdgcn_ballot_w64((u32)tk < (u32)NGRP) & 0xffu;
        if (!m8) break;
        const int rt = (s + wave) & 7;
        vic = (rt + __builtin_ctz((m8 | m8 << 8) >> rt)) & 7;
        DRAW();
        g = __builtin_amdgcn_readlane(tk, vic);
      }
      if (g >= (u32)NGRP) break;
      const int o = vic * CHUNK + NS * (NW * TILE) + g * TILE;
      if (CFG_SP || vic == s) PACE();
      ISSUE(KS, rk, o);
      DRAW();
      TOUCH(VS);
      VTILE_RUN(VS);
      SB();
      if (CFG_SP || vic == s) PACE();
      ISSUE(VS, rv, o);
      TOUCH(KS);
      SCORE(KS);
      asm volatile("" : "+v"(sc));
      SB();
    }
  }
  const rsrc_t ro = mkrsrc(wo, NH * DH * DM * 2);
  const u32x4 woa = BLD(ro, lo * 2 * (DM * 2) + hi * 16, (h * DH + wave * 16) * (DM * 2) + s * 128, AUX_W);
  const u32x4 wob = BLD(ro, lo * 2 * (DM * 2) + hi * 16, (h * DH + wave * 16 + 1) * (DM * 2) + s * 128, AUX_W);
  asm volatile("" ::: "memory");
  if (lane == 0) l_q[67] = 1;
  SB();
  VTILE_RUN(VS);
  l += dpp<XOR1>(l); l += dpp<XOR2>(l);
  STAMP(S_loopend);

  {
    const float mw = xrow_max(m);
    const float f = __builtin_amdgcn_exp2f(m - mw);
    l = xrow_sum(l * f);
    float a[8];
#pragma unroll
    for (int k = 0; k < 4; ++k) { a[2 * k] = xrow_sum(acc[k][0] * f); a[2 * k + 1] = xrow_sum(acc[k][1] * f); }
    if (r == 0) {
      *(f32x4*)(l_acc + wave * 132 + c * 8) = f32x4{a[0], a[1], a[2], a[3]};
      *(f32x4*)(l_acc + wave * 132 + c * 8 + 4) = f32x4{a[4], a[5], a[6], a[7]};
      if (c == 0) *(f32x2*)(l_acc + wave * 132 + 128) = f32x2{mw, l};
    }
  }
  lds_barrier();
  {
    const int sob = WS_O + h * 8 * 2048;
    u32x4 go = BLD(rw, hi * 2048 + lo * 16, sob + wave * 128, AUX_SC1);
    u32x4 gm = BLD(rw, hi * 2048, sob + 1024, AUX_SC1);
    asm volatile("" ::: "memory");
    f32x2 wa[4], wb[4];
#pragma unroll
    for (int k = 0; k < 4; ++k) { wa[k] = f32x2{lo16(woa[k]), hi16(woa[k])}; wb[k] = f32x2{lo16(wob[k]), hi16(wob[k])}; }
    asm volatile("" : "+v"(wa[0]), "+v"(wa[1]), "+v"(wa[2]), "+v"(wa[3]), "+v"(wb[0]), "+v"(wb[1]), "+v"(wb[2]), "+v"(wb[3]));
    SB();
    while (ANY(BAD(go) | BAD(gm))) {
      __builtin_amdgcn_s_sleep(CFG_SLEEP);
      go = BLD(rw, hi * 2048 + lo * 16, sob + wave * 128, AUX_SC1);
      gm = BLD(rw, hi * 2048, sob + 1024, AUX_SC1);
      asm volatile("" ::: "memory");
    }
    STAMP(S_opoll);
    const float mj = asf(gm[0]);
    const float M = xrow_max(fmaxf(mj, dpp<XOR8>(mj)));
    const float e = __builtin_amdgcn_exp2f(mj - M);
    const float L = hi8_sum(e * asf(gm[2]));
    const float rl = __builtin_amdgcn_rcpf(L);
    const float oa = hi8_sum(e * asf(go[0])) * rl, ob = hi8_sum(e * asf(go[2])) * rl;
    float y[8];
#pragma unroll
    for (int k = 0; k < 4; ++k) {
      const f32x2 t = __builtin_elementwise_fma(f32x2{ob, ob}, wb[k], f32x2{oa, oa} * wa[k]);
      y[2 * k] = t[0]; y[2 * k + 1] = t[1];
    }
#pragma unroll
    for (int k = 0; k < 8; ++k) { y[k] += dpp<XOR1>(y[k]); y[k] += dpp<XOR2>(y[k]); y[k] += dpp<XOR7>(y[k]); }
    if (lo == 0) {
      *(f32x4*)(l_y + wave * 64 + hi * 8) = f32x4{y[0], y[1], y[2], y[3]};
      *(f32x4*)(l_y + wave * 64 + hi * 8 + 4) = f32x4{y[4], y[5], y[6], y[7]};
    }
  }
  lds_barrier();
  STAMP(S_bar3);
  FLUSH(wave);
}

extern "C" void kernel_launch(void* const* d_in, const int* in_sizes, int n_in, void* d_out, int out_size, void* d_ws,
                              size_t ws_size, hipStream_t stream) {
  fused_kernel<<<NWG, NTHR, 0, stream>>>((const u16*)d_in[0], (const u16*)d_in[1], (const u16*)d_in[2], (const u16*)d_in[3],
                                         (const u16*)d_in[4], (const u16*)d_in[6], (const u16*)d_in[7], (const u16*)d_in[8],
                                         (const u16*)d_in[9], (u16*)d_out, (unsigned char*)d_ws);
}
```

```cpp
#include <hip/hip_runtime.h>
#include <stdint.h>

#define CFG_NS 62
#define CFG_NB 8
#define CFG_D 2
#define CFG_GS0 30
#define CFG_GS1 10
#define CFG_TAU 436
#define CFG_TOFF 50
#define CFG_ROFF 50
#define CFG_SP 0
#define CFG_SLEEP 1
#define CFG_FDELAY 0

#define CFG_L 0
#define CFG_PH 7
#define CFG_CB 0
#define CFG_LK 3
#define CFG_BO -2
#define CFG_TH -100
#define CFG_CU 0
#define CFG_Q 1
#define CFG_ABS 1
#define CFG_VT 10
#define CFG_TR 1
#define CFG_RC 0
#define CFG_RS 0
typedef unsigned int u32;
typedef unsigned long long u64;
typedef unsigned short u16;
typedef u32 u32x4 __attribute__((ext_vector_type(4)));
typedef float f32x4 __attribute__((ext_vector_type(4)));
typedef float f32x2 __attribute__((ext_vector_type(2)));
typedef __bf16 bf16x2 __attribute__((ext_vector_type(2)));
typedef __amdgpu_buffer_rsrc_t rsrc_t;

constexpr int DM = 512, NH = 32, SK = 65536, DH = 128, FF = 1792;
constexpr int NWG = 256, NW = 8, NSV = 3, NTHR = (NW + NSV) * 64;
constexpr int CHUNK = SK / 8 * DH * 2;
constexpr int WS_GEN = 0;
constexpr int WS_CT = 8192;
constexpr int WS_B = 16384;
constexpr int WS_Q = WS_B + NWG * 512;
constexpr int WS_O = WS_Q + NWG * 2048;
constexpr int WS_Y = WS_O + NWG * 2048;
constexpr int WS_BYTES = WS_Y + NWG * 512;
constexpr int AUX_NT = 2, AUX_W = 2, AUX_SC1 = 16, AUX_ST = 16;

__device__ __forceinline__ rsrc_t mkrsrc(const void* p, int bytes) {
  return __builtin_amdgcn_make_buffer_rsrc((void*)p, 0, bytes, 0x00020000);
}
__device__ __forceinline__ float asf(u32 x) { return __builtin_bit_cast(float, x); }
__device__ __forceinline__ u32 asu(float x) { return __builtin_bit_cast(u32, x); }
__device__ __forceinline__ float lo16(u32 x) { return asf(x << 16); }
__device__ __forceinline__ float hi16(u32 x) { return asf(x & 0xffff0000u); }
__device__ __forceinline__ float rbf(float x) { __bf16 b = (__bf16)x; return (float)b; }
__device__ __forceinline__ u32 pk2(float a, float b) { return __builtin_bit_cast(u32, bf16x2{(__bf16)a, (__bf16)b}); }
__device__ __forceinline__ float dot2(u32 a, u32 b, float acc) {
  return __builtin_amdgcn_fdot2_f32_bf16(__builtin_bit_cast(bf16x2, a), __builtin_bit_cast(bf16x2, b), acc, false);
}
template <int CTRL> __device__ __forceinline__ float dpp(float x) {
  return __builtin_bit_cast(float, __builtin_amdgcn_mov_dpp(__builtin_bit_cast(int, x), CTRL, 0xf, 0xf, true));
}
constexpr int XOR1 = 0xB1, XOR2 = 0x4E, XOR7 = 0x141, XOR8 = 0x128, ROR4 = 0x124;
__device__ __forceinline__ float row16_sum(float x) {
  x += dpp<XOR8>(x); x += dpp<XOR7>(x); x += dpp<XOR2>(x); x += dpp<XOR1>(x); return x;
}
__device__ __forceinline__ float xrow_sum(float x) {
  auto s = __builtin_amdgcn_permlane16_swap(asu(x), asu(x), false, false);
  x = asf(s[0]) + asf(s[1]);
  auto t = __builtin_amdgcn_permlane32_swap(asu(x), asu(x), false, false);
  return asf(t[0]) + asf(t[1]);
}
__device__ __forceinline__ float xrow_max(float x) {
  auto s = __builtin_amdgcn_permlane16_swap(asu(x), asu(x), false, false);
  x = fmaxf(asf(s[0]), asf(s[1]));
  auto t = __builtin_amdgcn_permlane32_swap(asu(x), asu(x), false, false);
  return fmaxf(asf(t[0]), asf(t[1]));
}
__device__ __forceinline__ float hi8_sum(float x) { x += dpp<XOR8>(x); return xrow_sum(x); }
__device__ __forceinline__ float wave_sum(float x) { return xrow_sum(row16_sum(x)); }
__device__ __forceinline__ void lds_barrier() {
  asm volatile("s_waitcnt lgkmcnt(0)" ::: "memory");
  __builtin_amdgcn_s_barrier();
  asm volatile("" ::: "memory");
}
#define BLD(r, vo, so, aux) __builtin_amdgcn_raw_buffer_load_b128(r, vo, so, aux)
#define BST(v, r, vo, aux) __builtin_amdgcn_raw_buffer_store_b128(v, r, vo, 0, aux)
#define BAD(v) (((v)[1] ^ tlo) | ((v)[3] ^ thi))
#define ANY(x) (__builtin_amdgcn_ballot_w64((x) != 0) != 0)
#define SB() __builtin_amdgcn_sched_barrier(0)

#define STAMP(i) do { } while (0)
#define FLUSH(w) do { } while (0)

__device__ __forceinline__ f32x2 hi8_fold(const f32x2* a) {
  float b[4];
#pragma unroll
  for (int i = 0; i < 4; ++i) {
    const auto t = __builtin_amdgcn_permlane32_swap(asu(a[i >> 1][i & 1]), asu(a[2 + (i >> 1)][i & 1]), false, false);
    b[i] = asf(t[0]) + asf(t[1]);
  }
  f32x2 o;
#pragma unroll
  for (int i = 0; i < 2; ++i) {
    const auto t = __builtin_amdgcn_permlane16_swap(asu(b[i]), asu(b[2 + i]), false, false);
    const float v = asf(t[0]) + asf(t[1]);
    o[i] = v + dpp<XOR8>(v);
  }
  return o;
}
__device__ __forceinline__ float half_sum(float x) {
  const auto t = __builtin_amdgcn_permlane32_swap(asu(x), asu(x), false, false);
  return asf(t[0]) + asf(t[1]);
}

__global__ void __launch_bounds__(NTHR)
fused_kernel(const u16* x, const u16* K, const u16* V, const u16* wq, const u16* wo, const u16* w1, const u16* b1,
             const u16* w2, const u16* b2, u16* out, unsigned char* ws
) {
  constexpr int U = 4, NB = CFG_NB, NS = CFG_NS, D = CFG_D, FG = FF / NH;
  constexpr int TILE = U * 1024, NT = CHUNK / (TILE * NW), NGRP = (NT - NS) * NW;
  static_assert(NS % D == 0 && NB % D == 0 && NS >= NB + 2 * D && NS <= NT, "");
  __shared__ __attribute__((aligned(16))) float lds[24 * 1024];
  float* const l_y = lds;
  float* const l_acc = lds + 512;
  float* const l_h = l_acc + 8 * 132;
  u32* const l_q = (u32*)(l_h + 64);
  float* const l_s = l_h + 64 + 128;

  const int wg = blockIdx.x, tid = threadIdx.x, lane = tid & 63;
  const int wv = __builtin_amdgcn_readfirstlane(tid >> 6), wave = (wv - NSV) & 7;
  const int r = lane >> 4, c = lane & 15, hi = lane >> 3, lo = lane & 7;
  const int h = wg >> 3, s = wg & 7;
  const int vo = lane * 16;
  const rsrc_t rw = mkrsrc(ws, WS_BYTES);

  STAMP(S_start);
  asm volatile("" :: "s"(K), "s"(b2), "s"(out), "s"(ws));
  const rsrc_t rk = mkrsrc((const char*)K + (size_t)h * (8 * CHUNK), wv < NSV ? 0 : 8 * CHUNK);
  constexpr int RSTEP = CFG_L == 0 ? NW * TILE : 8 * NW * TILE, SSTEP = CFG_L == 0 ? CHUNK : CFG_L == 1 ? NW * TILE : TILE, WSTEP = CFG_L == 2 ? 8 * TILE : TILE;
  const int sb = s * SSTEP + wave * WSTEP;
  u32x4 tb[NB][U];
#define ISSUE(sl, rs_, so_) do { _Pragma("unroll") for (int u = 0; u < U; ++u) tb[sl][u] = BLD(rs_, vo + u * 1024, so_, AUX_NT); asm volatile("" ::: "memory"); } while (0)
#define TOUCH(sl) do { _Pragma("unroll") for (int u = 0; u < U; ++u) asm volatile("" : "+v"(tb[sl][u]) :: "memory"); } while (0)
#define ST(t) (sb + (t) * RSTEP)
  ISSUE(0, rk, ST(0));
  const u32 tbase = (u32)__builtin_amdgcn_s_memrealtime();
  if (tid < 6) l_q[66 + tid] = 0;
  if (tid == NSV * 64) l_q[72] = tbase;
  lds_barrier();
  SB();

  if (__builtin_expect(wv < NSV, 1)) {
    const rsrc_t rx = mkrsrc(x, DM * 2);
    const bool ffn = wv == 2;
    const rsrc_t ra = ffn ? mkrsrc(w1, DM * FF * 2) : mkrsrc(wq, DM * NH * DH * 2);
    const int pitch = ffn ? FF * 2 : NH * DH * 2;
    const int voa = (ffn && lo == 7) ? DM * FF * 2 : hi * 8 * pitch + lo * 16;
    const int soa = s * 64 * pitch + (ffn ? h * (FG * 2) : h * 256 + wv * 128);
    if (ffn) for (int i = 0; i < CFG_FDELAY; ++i) __builtin_amdgcn_s_sleep(32);
    asm volatile("" ::: "memory");
    const u32x4 xs = BLD(rx, vo, 0, 0);
    const u32x4 xv = BLD(rx, hi * 16, s * 128, 0);
    u32x4 wa[8];
#pragma unroll
    for (int j = 0; j < 8; ++j) wa[j] = BLD(ra, voa, soa + j * pitch, AUX_W);
    const auto gen2 = __builtin_amdgcn_raw_buffer_load_b64(rw, 0, WS_GEN, AUX_SC1);
    u32 ct0 = 0;
    if (s == 0 && tid < 8) ct0 = __hip_atomic_exchange((u32*)(ws + WS_CT + h * 256) + tid, 0u, __ATOMIC_RELAXED, __HIP_MEMORY_SCOPE_AGENT);
    SB();
    const u32 b1v = __builtin_amdgcn_raw_buffer_load_b32(mkrsrc(b1, ffn ? FF * 2 : 0), (lane & 31) < FG / 2 ? (lane & 31) * 4 : FF * 2, h * (FG * 2), 0);
    const u32 xo = __builtin_amdgcn_raw_buffer_load_b32(rx, lo * 4, h * 32, 0);
    const u32 bo = __builtin_amdgcn_raw_buffer_load_b32(mkrsrc(b2, DM * 2), lo * 4, h * 32, 0);
    asm volatile("" ::: "memory");
    SB();
    float xn[8];
    {
      float e[8];
#pragma unroll
      for (int k = 0; k < 4; ++k) { e[2 * k] = lo16(xs[k]); e[2 * k + 1] = hi16(xs[k]); }
      float sum = ((e[0] + e[1]) + (e[2] + e[3])) + ((e[4] + e[5]) + (e[6] + e[7]));
      const float mean = wave_sum(sum) * (1.0f / DM);
      float sq = 0.f;
#pragma unroll
      for (int k = 0; k < 8; ++k) { const float d = e[k] - mean; sq = fmaf(d, d, sq); }
      const float var = wave_sum(sq) * (1.0f / DM);
      const float mean_b = rbf(mean);
      const float sd = rbf(__builtin_amdgcn_sqrtf(rbf(rbf(var) + 1e-5f)));
      const float rstd = __builtin_amdgcn_rcpf(sd);
#pragma unroll
      for (int k = 0; k < 4; ++k) { xn[2 * k] = rbf(rbf(lo16(xv[k]) - mean_b) * rstd); xn[2 * k + 1] = rbf(rbf(hi16(xv[k]) - mean_b) * rstd); }
    }
    asm volatile("" : "+v"(xn[0]), "+v"(xn[1]), "+v"(xn[2]), "+v"(xn[3]), "+v"(xn[4]), "+v"(xn[5]), "+v"(xn[6]), "+v"(xn[7]));
    SB();
    STAMP(S_ln);
    f32x2 a[4] = {{0, 0}, {0, 0}, {0, 0}, {0, 0}};
#pragma unroll
    for (int j = 0; j < 8; ++j)
#pragma unroll
      for (int k = 0; k < 4; ++k) a[k] = __builtin_elementwise_fma(f32x2{xn[j], xn[j]}, f32x2{lo16(wa[j][k]), hi16(wa[j][k])}, a[k]);
    const f32x2 part = hi8_fold(a);
    const u64 gen = ((u64)(u32)__builtin_amdgcn_readfirstlane(gen2[1]) << 32) | (u32)__builtin_amdgcn_readfirstlane(gen2[0]);
    const u64 tag = gen * 6364136223846793005ull + 1442695040888963407ull;
    const u32 tlo = (u32)tag, thi = (u32)(tag >> 32);
    asm volatile("" :: "v"(ct0));
    STAMP(S_part);
    const int vop = (lo * 4 + r) * 16;
    BST((u32x4{asu(part[0]), tlo, asu(part[1]), thi}), rw, (lane & 8) ? WS_BYTES : WS_Q + wg * 2048 + wv * 512 + vop, AUX_ST);
    if (CFG_CB) BST((u32x4{tbase, tlo, tbase, thi}), rw, (tid == 0) ? WS_Q + wg * 2048 + 1536 : WS_BYTES, AUX_ST);
    STAMP(S_qpub);
    f32x2 g;
    {
      const int vog = (lane >> 5) * 2048 + (lane & 31) * 16, sog = WS_Q + h * 8 * 2048 + wv * 512;
      u32x4 gq[4], gt = {0, tlo, 0, thi};
      if (ffn) __builtin_amdgcn_s_sleep(100); else if (CFG_GS0) __builtin_amdgcn_s_sleep(CFG_GS0);
      while (true) {
#pragma unroll
        for (int j = 0; j < 4; ++j) gq[j] = BLD(rw, vog, sog + j * 4096, AUX_SC1);
        if (CFG_CB) gt = BLD(rw, (lane & 7) * 2048, WS_Q + h * 8 * 2048 + 1536, AUX_SC1);
        asm volatile("" : "+v"(gq[0]), "+v"(gq[1]), "+v"(gq[2]), "+v"(gq[3]), "+v"(gt) :: "memory");
        if (!ANY(BAD(gq[0]) | BAD(gq[1]) | BAD(gq[2]) | BAD(gq[3]) | BAD(gt))) break;
        if (CFG_GS1) __builtin_amdgcn_s_sleep(CFG_GS1);
      }
      if (CFG_CB) {
        int d = (int)(gt[0] - tbase), b = 0;
        for (int i = 0; i < 8; ++i) { const int e = __builtin_amdgcn_readlane(d, i); b = CFG_CB == 1 ? (e < b ? e : b) : CFG_CB == 2 ? (e > b ? e : b) : b + e; }
        if (CFG_CB == 3) b = b >> 3;
        if (tid == 0) l_q[68] = tbase + b;
      }
      g[0] = half_sum((asf(gq[0][0]) + asf(gq[1][0])) + (asf(gq[2][0]) + asf(gq[3][0])));
      g[1] = half_sum((asf(gq[0][2]) + asf(gq[1][2])) + (asf(gq[2][2]) + asf(gq[3][2])));
    }
    STAMP(S_gath);
    if (!ffn) {
      if (lane < 32) l_q[wv * 32 + lane] = pk2(g[0], g[1]);
      if (lane == 0) { l_q[64] = tlo; l_q[65] = thi; }
      asm volatile("s_waitcnt lgkmcnt(0)" ::: "memory");
      if (lane == 0) __hip_atomic_fetch_add(l_q + 66, 1u, __ATOMIC_RELAXED, __HIP_MEMORY_SCOPE_WORKGROUP);
      STAMP(S_qready);
      if (CFG_VT && wv == 1) {
        for (int i = 0; i < CFG_VT; ++i) __builtin_amdgcn_s_sleep(127);
        const u32 t = __builtin_amdgcn_raw_buffer_load_b32(mkrsrc((const char*)V + (size_t)wg * CHUNK, CHUNK), (lane & 1) * (CHUNK - 4), 0, AUX_NT);
        asm volatile("" :: "v"(t));
      }
      lds_barrier();
      STAMP(S_bar2);
      if (wv == 0) {
        f32x2 ml[8], ov[8];
#pragma unroll
        for (int w = 0; w < 8; ++w) { ml[w] = *(const f32x2*)(l_acc + w * 132 + 128); ov[w] = *(const f32x2*)(l_acc + w * 132 + lane * 2); }
        float Mg = ml[0][0], Lg = 0.f;
#pragma unroll
        for (int w = 1; w < 8; ++w) Mg = fmaxf(Mg, ml[w][0]);
        f32x2 o = {0, 0};
#pragma unroll
        for (int w = 0; w < 8; ++w) {
          const float f = __builtin_amdgcn_exp2f(ml[w][0] - Mg);
          Lg = fmaf(f, ml[w][1], Lg);
          o = __builtin_elementwise_fma(f32x2{f, f}, ov[w], o);
        }
        BST((u32x4{asu(o[0]), tlo, asu(o[1]), thi}), rw, WS_O + wg * 2048 + vo, AUX_ST);
        BST((u32x4{asu(Mg), tlo, asu(Lg), thi}), rw, lane == 0 ? WS_O + wg * 2048 + 1024 : WS_BYTES, AUX_ST);
        STAMP(S_opub);
      }
      lds_barrier();
      if (wv == 0) {
        const float* src = l_y + (lane >> 5) * 256 + (lane & 31) * 2;
        const f32x2 t = (*(const f32x2*)src + *(const f32x2*)(src + 64)) + (*(const f32x2*)(src + 128) + *(const f32x2*)(src + 192));
        BST((u32x4{asu(half_sum(t[0])), tlo, asu(half_sum(t[1])), thi}), rw, lane < 32 ? WS_Y + wg * 512 + vo : WS_BYTES, AUX_ST);
        STAMP(S_ypub);
      }
    } else {
      {
        float h0 = rbf(rbf(g[0]) + lo16(b1v)), h1 = rbf(rbf(g[1]) + hi16(b1v));
        h0 = h0 > 0.f ? h0 : 0.f; h1 = h1 > 0.f ? h1 : 0.f;
        if (lane < 32) *(f32x2*)(l_h + lane * 2) = f32x2{rbf(h0 * h0), rbf(h1 * h1)};
        asm volatile("s_waitcnt lgkmcnt(0)" ::: "memory");
        while (__hip_atomic_load(l_q + 67, __ATOMIC_RELAXED, __HIP_MEMORY_SCOPE_WORKGROUP) == 0) __builtin_amdgcn_s_sleep(4);
        asm volatile("" ::: "memory");
        const rsrc_t r2 = mkrsrc(w2, FF * DM * 2);
        u32x4 wb[7];
#pragma unroll
        for (int j = 0; j < 7; ++j) wb[j] = BLD(r2, hi * (DM * 2) + lo * 16, (h * FG + j * 8) * (DM * 2) + s * 128, AUX_W);
        asm volatile("" ::: "memory");
        lds_barrier();
        f32x2 y[4] = {{0, 0}, {0, 0}, {0, 0}, {0, 0}};
#pragma unroll
        for (int j = 0; j < 7; ++j) {
          const float hh = l_h[j * 8 + hi];
#pragma unroll
          for (int k = 0; k < 4; ++k) y[k] = __builtin_elementwise_fma(f32x2{hh, hh}, f32x2{lo16(wb[j][k]), hi16(wb[j][k])}, y[k]);
        }
        const f32x2 t = hi8_fold(y);
        BST((u32x4{asu(t[0]), tlo, asu(t[1]), thi}), rw, (lane & 8) ? WS_BYTES : WS_B + wg * 512 + vop, AUX_ST);
        STAMP(S_bpub);
      }
      const bool fin = s == (h & 7);
      const int voy = hi * 4096 + lo * 16, soy = (h >> 2) * 512 + (h & 3) * 128;
      lds_barrier();
      if (fin) {
        u32x4 pb[4], pc[4];
        while (true) {
#pragma unroll
          for (int j = 0; j < 4; ++j) pc[j] = BLD(rw, voy, WS_B + soy + j * 32768, AUX_SC1);
#pragma unroll
          for (int j = 0; j < 4; ++j) pb[j] = BLD(rw, voy, WS_Y + soy + j * 32768, AUX_SC1);
          asm volatile("" : "+v"(pc[0]), "+v"(pc[1]), "+v"(pc[2]), "+v"(pc[3]) :: "memory");
          if (!ANY(BAD(pc[0]) | BAD(pc[1]) | BAD(pc[2]) | BAD(pc[3]))) break;
          __builtin_amdgcn_s_sleep(CFG_SLEEP);
        }
        const float c0 = hi8_sum((asf(pc[0][0]) + asf(pc[1][0])) + (asf(pc[2][0]) + asf(pc[3][0])));
        const float c1 = hi8_sum((asf(pc[0][2]) + asf(pc[1][2])) + (asf(pc[2][2]) + asf(pc[3][2])));
        float B0 = rbf(rbf(c0) + lo16(bo)), B1 = rbf(rbf(c1) + hi16(bo));
        asm volatile("" : "+v"(B0), "+v"(B1), "+v"(pb[0]), "+v"(pb[1]), "+v"(pb[2]), "+v"(pb[3]));
        while (ANY(BAD(pb[0]) | BAD(pb[1]) | BAD(pb[2]) | BAD(pb[3]))) {
#pragma unroll
          for (int j = 0; j < 4; ++j) pb[j] = BLD(rw, voy, WS_Y + soy + j * 32768, AUX_SC1);
          asm volatile("" : "+v"(pb[0]), "+v"(pb[1]), "+v"(pb[2]), "+v"(pb[3]) :: "memory");
        }
        STAMP(S_ypoll);
        const float A0 = hi8_sum((asf(pb[0][0]) + asf(pb[1][0])) + (asf(pb[2][0]) + asf(pb[3][0])));
        const float A1 = hi8_sum((asf(pb[0][2]) + asf(pb[1][2])) + (asf(pb[2][2]) + asf(pb[3][2])));
        const u32 res = pk2(rbf(lo16(xo) + rbf(A0)) + B0, rbf(hi16(xo) + rbf(A1)) + B1);
        if (lane < 8) ((u32*)out)[h * 8 + lane] = res;
        if (h == 0 && lane == 8) __hip_atomic_store((u64*)(ws + WS_GEN), tag, __ATOMIC_RELAXED, __HIP_MEMORY_SCOPE_AGENT);
        STAMP(S_final);
      }
    }
    FLUSH(wv);
    return;
  }

#define CFG_WS 16
  const int wph = CFG_PH == 0 ? ((wave + h) & 7) * CFG_TAU / 8 : CFG_PH == 1 ? 0 : CFG_PH == 2 ? wave * CFG_TAU / 8 : wave * CFG_TAU / CFG_WS;
  const int gph = CFG_PH == 0 ? 0 : CFG_PH == 1 ? h * CFG_TAU / 32 : CFG_PH == 2 ? (h & 3) * CFG_TAU / 32 : CFG_PH == 3 ? ((s + h) & 7) * CFG_TAU / 8
    : CFG_PH == 5 ? ((s * 4 + h) & 31) * CFG_TAU / 32 : CFG_PH == 6 ? h * CFG_TAU / 32 : CFG_PH == 7 ? wg * CFG_TAU / 256 : ((s * 33 + h * 8) & 255) * CFG_TAU / 256;
  u32 due = (tbase + CFG_ROFF) * 4 + CFG_TAU + wph + gph;
  if (CFG_ABS) {
    const u32 x = (__builtin_amdgcn_readfirstlane(l_q[72]) + CFG_ROFF) * 4 + CFG_TAU;
    due = x + (CFG_TAU - 1 - (x + CFG_TAU - 1 - gph) % CFG_TAU) + wph;
  }
#define PACE() do { while ((int)((u32)__builtin_amdgcn_s_memrealtime() * 4 - due) < 0) __builtin_amdgcn_s_sleep(1); due += CFG_TAU; } while (0)
  u32 need = 0;
#define ARRIVE() do { if (CFG_LK == 3) { const int off_ = __builtin_amdgcn_readfirstlane(__hip_atomic_load(l_q + 71, __ATOMIC_RELAXED, __HIP_MEMORY_SCOPE_WORKGROUP)); \
      const int late_ = (int)((u32)__builtin_amdgcn_s_memrealtime() * 4 - due) - (CFG_TH); \
      if (late_ > off_) { if (lane == 0) __hip_atomic_fetch_max((int*)l_q + 71, (late_ / CFG_TAU + 1) * CFG_TAU, __ATOMIC_RELAXED, __HIP_MEMORY_SCOPE_WORKGROUP); } asm volatile("" ::: "memory"); } \
    if (CFG_LK == 1) { if (lane == 0) __hip_atomic_fetch_add(l_q + 70, 1u, __ATOMIC_RELAXED, __HIP_MEMORY_SCOPE_WORKGROUP); need += NW; asm volatile("" ::: "memory"); } } while (0)
  int cu = 0;
#define PACEL() do { if (CFG_LK == 3) { while (true) { const u32 off_ = __hip_atomic_load(l_q + 71, __ATOMIC_RELAXED, __HIP_MEMORY_SCOPE_WORKGROUP); const u32 now_ = (u32)__builtin_amdgcn_s_memrealtime() * 4; \
      if ((int)(now_ - due - __builtin_amdgcn_readfirstlane(off_)) >= 0) break; __builtin_amdgcn_s_sleep(1); } due += CFG_TAU; } else if (CFG_LK == 2) { const int late_ = (int)((u32)__builtin_amdgcn_s_memrealtime() * 4 - due); if (late_ > CFG_TH) due += ((late_ - CFG_TH) / (CFG_TAU / CFG_Q) + 1) * (CFG_TAU / CFG_Q); PACE(); } else if (CFG_LK) { bool first_ = CFG_BO != -1; while (true) { \
      const u64 co_ = __hip_atomic_load((u64*)(l_q + 70), __ATOMIC_RELAXED, __HIP_MEMORY_SCOPE_WORKGROUP); const u32 now_ = (u32)__builtin_amdgcn_s_memrealtime() * 4; \
      if (__builtin_amdgcn_readfirstlane((u32)co_) >= need) { \
        int off_ = __builtin_amdgcn_readfirstlane((u32)(co_ >> 32)) - cu; off_ = off_ > 0 ? off_ : 0; \
        const int late_ = (int)(now_ - due) - off_; \
        if (first_ && late_ > CFG_TH) { if (lane == 0) __hip_atomic_fetch_max((int*)l_q + 71, CFG_BO >= 0 ? (int)(now_ - due) + cu + CFG_BO : cu + (((int)(now_ - due) - CFG_TH) / (CFG_TAU / CFG_Q) + 1) * (CFG_TAU / CFG_Q), __ATOMIC_RELAXED, __HIP_MEMORY_SCOPE_WORKGROUP); asm volatile("" ::: "memory"); } \
        else if (late_ >= 0) break; \
        first_ = false; } \
      __builtin_amdgcn_s_sleep(1); } due += CFG_TAU; cu += CFG_CU; } else PACE(); } while (0)
  const rsrc_t rv = mkrsrc((const char*)V + (size_t)h * (8 * CHUNK), 8 * CHUNK);
  asm volatile("" ::: "memory");
  SB();
  STAMP(S_issued);
#pragma unroll
  for (int t = 1; t < NB; ++t) {
    if (CFG_RC && t >= CFG_RC) { TOUCH(t - CFG_RC); if (t - CFG_RC <= 4) STAMP(S_t0 + t - CFG_RC); if (t >= CFG_RS) ARRIVE(); PACEL(); }
    else { PACE(); if (!CFG_RC && t <= 5) STAMP(S_t0 + t - 1); }
    ISSUE(t, rk, ST(t));
    SB();
  }
  due += (CFG_TOFF - CFG_ROFF) * 4;
  while (__hip_atomic_load(l_q + 66, __ATOMIC_RELAXED, __HIP_MEMORY_SCOPE_WORKGROUP) != 2) __builtin_amdgcn_s_sleep(1);
  asm volatile("" ::: "memory");
  STAMP(S_flag);
  if (CFG_CB) due += (__builtin_amdgcn_readfirstlane(l_q[68]) - tbase) * 4;
  const u32x4 qp = *(const u32x4*)(l_q + c * 4);
  const u32 tlo = __builtin_amdgcn_readfirstlane(l_q[64]), thi = __builtin_amdgcn_readfirstlane(l_q[65]);
  const bool q0 = lane & 1, q1 = lane & 2;
  float* const ls = l_s + wave * (NS * 16) + r * 4 + (lane & 3);
  float m = -__builtin_inff(), l = 0.f, sc;
  f32x2 acc[4] = {{0, 0}, {0, 0}, {0, 0}, {0, 0}};
#define SCORE(sl) do { \
    float d[U]; \
    _Pragma("unroll") for (int u = 0; u < U; ++u) { \
      d[u] = dot2(tb[sl][u][0], qp[0], 0.f); d[u] = dot2(tb[sl][u][1], qp[1], d[u]); d[u] = dot2(tb[sl][u][2], qp[2], d[u]); d[u] = dot2(tb[sl][u][3], qp[3], d[u]); } \
    const float e0 = (q0 ? d[1] : d[0]) + dpp<XOR1>(q0 ? d[0] : d[1]), e1 = (q0 ? d[3] : d[2]) + dpp<XOR1>(q0 ? d[2] : d[3]); \
    float f = (q1 ? e1 : e0) + dpp<XOR2>(q1 ? e0 : e1); \
    f += dpp<XOR8>(f); f += dpp<ROR4>(f); \
    sc = rbf(rbf(f) * 0.08838834764831845f) * 1.4426950408889634f; \
  } while (0)
#define KTILE(sl, t) do { SCORE(sl); ls[(t) * 16] = sc; m = fmaxf(m, sc); asm volatile("" : "+v"(m) :: "memory"); } while (0)
#define ACCUM(sl) do { \
    const float p = __builtin_amdgcn_exp2f(sc - m); l += p; \
    const float pu[U] = {dpp<0x00>(p), dpp<0x55>(p), dpp<0xAA>(p), dpp<0xFF>(p)}; \
    _Pragma("unroll") for (int u = 0; u < U; ++u) \
      _Pragma("unroll") for (int k = 0; k < 4; ++k) acc[k] = __builtin_elementwise_fma(f32x2{pu[u], pu[u]}, f32x2{lo16(tb[sl][u][k]), hi16(tb[sl][u][k])}, acc[k]); \
    asm volatile("" : "+v"(acc[0]), "+v"(acc[1]), "+v"(acc[2]), "+v"(acc[3]), "+v"(l)); \
  } while (0)
#define VTILE(sl, t) do { sc = ls[(t) * 16]; ACCUM(sl); } while (0)
#define VTILE_RUN(sl) do { \
    float mn = fmaxf(sc, dpp<XOR1>(sc)); mn = fmaxf(mn, dpp<XOR2>(mn)); mn = fmaxf(mn, m); \
    const float alpha = __builtin_amdgcn_exp2f(m - mn); \
    m = mn; l *= alpha; \
    _Pragma("unroll") for (int k = 0; k < 4; ++k) acc[k] *= f32x2{alpha, alpha}; \
    ACCUM(sl); \
  } while (0)
  static_assert(D == 2, "");
  KTILE(0, 0); SB(); if (CFG_RC) TOUCH(NB - 2); ARRIVE(); PACEL(); ISSUE(0, rk, ST(NB));
  KTILE(1, 1);
#pragma unroll
  for (int t = 2; t < 2 + (NB - 2) / 2; ++t) KTILE(t, t);
  SB(); if (CFG_RC) TOUCH(NB - 1); ARRIVE(); PACEL(); ISSUE(1, rk, ST(NB + 1));
#pragma unroll
  for (int t = 2 + (NB - 2) / 2; t < NB; ++t) KTILE(t, t);
  SB();
  STAMP(S_t5);
  for (int t = NB; t < NS - D; t += D) {
#pragma unroll
    for (int ph = 0; ph < D; ++ph) {
      TOUCH(ph); ARRIVE();
      KTILE(ph, t + ph);
      SB();
      PACEL();
      ISSUE(ph, rk, ST(t + ph + D));
    }
  }
#pragma unroll
  for (int ph = 0; ph < D; ++ph) {
    TOUCH(ph); ARRIVE();
    KTILE(ph, NS - D + ph);
    SB();
    PACEL();
    ISSUE(ph, rv, ST(ph));
  }
  STAMP(S_kend);
  m = fmaxf(m, dpp<XOR1>(m)); m = fmaxf(m, dpp<XOR2>(m)); m = xrow_max(m);
  for (int t = 0; t < NS - 2 * D; t += D) {
#pragma unroll
    for (int ph = 0; ph < D; ++ph) {
      TOUCH(ph); ARRIVE();
      VTILE(ph, t + ph);
      SB();
      PACEL();
      ISSUE(ph, rv, ST(t + ph + D));
    }
  }
  STAMP(S_statend);
  constexpr int KS = D - 2, VS = D - 1;
  {
    int cvo = lane < 8 ? WS_CT + h * 256 + lane * 4 : WS_BYTES;
    int vic = s;
    asm volatile("" : "+v"(cvo));
    int tk = 0;
#define DRAW() do { tk = __builtin_amdgcn_raw_ptr_buffer_atomic_add_i32((int)(lane == vic), rw, cvo, 0, 0); asm volatile("" ::: "memory"); } while (0)
#pragma unroll
    for (int ph = 0; ph < D; ++ph) {
      TOUCH(ph); ARRIVE();
      VTILE(ph, NS - 2 * D + ph);
      SB();
      PACEL();
      ISSUE(ph, rv, ST(NS - D + ph));
      if (ph == D - 2) DRAW();
    }
#pragma unroll
    for (int ph = 0; ph < D - 1; ++ph) {
      TOUCH(ph);
      VTILE(ph, NS - D + ph);
    }
    if (CFG_LK & 1) { int off_ = __builtin_amdgcn_readfirstlane(l_q[71]) - cu; due += off_ > 0 ? off_ : 0; }
    sc = ls[(NS - 1) * 16];
    SB();
    while (true) {
      u32 g = __builtin_amdgcn_readlane(tk, vic);
      while (g >= (u32)NGRP) {
        const u32 m8 = (u32)__builtin_amdgcn_ballot_w64((u32)tk < (u32)NGRP) & 0xffu;
        if (!m8) break;
        const int rt = (s + wave) & 7;
        vic = (rt + __builtin_ctz((m8 | m8 << 8) >> rt)) & 7;
        DRAW();
        g = __builtin_amdgcn_readlane(tk, vic);
      }
      if (g >= (u32)NGRP) break;
      const int o = vic * SSTEP + (NS + (g >> 3)) * RSTEP + (g & 7) * WSTEP;
      if (CFG_SP || vic == s) PACE();
      ISSUE(KS, rk, o);
      DRAW();
      TOUCH(VS);
      VTILE_RUN(VS);
      SB();
      if (CFG_SP || vic == s) PACE();
      ISSUE(VS, rv, o);
      TOUCH(KS);
      SCORE(KS);
      asm volatile("" : "+v"(sc));
      SB();
    }
  }
  const rsrc_t ro = mkrsrc(wo, NH * DH * DM * 2);
  const u32x4 woa = BLD(ro, lo * 2 * (DM * 2) + hi * 16, (h * DH + wave * 16) * (DM * 2) + s * 128, AUX_W);
  const u32x4 wob = BLD(ro, lo * 2 * (DM * 2) + hi * 16, (h * DH + wave * 16 + 1) * (DM * 2) + s * 128, AUX_W);
  asm volatile("" ::: "memory");
  if (lane == 0) l_q[67] = 1;
  SB();
  VTILE_RUN(VS);
  l += dpp<XOR1>(l); l += dpp<XOR2>(l);
  STAMP(S_loopend);

  {
    const float mw = xrow_max(m);
    const float f = __builtin_amdgcn_exp2f(m - mw);
    l = xrow_sum(l * f);
    float a[8];
#pragma unroll
    for (int k = 0; k < 4; ++k) { a[2 * k] = xrow_sum(acc[k][0] * f); a[2 * k + 1] = xrow_sum(acc[k][1] * f); }
    if (r == 0) {
      *(f32x4*)(l_acc + wave * 132 + c * 8) = f32x4{a[0], a[1], a[2], a[3]};
      *(f32x4*)(l_acc + wave * 132 + c * 8 + 4) = f32x4{a[4], a[5], a[6], a[7]};
      if (c == 0) *(f32x2*)(l_acc + wave * 132 + 128) = f32x2{mw, l};
    }
  }
  lds_barrier();
  {
    const int sob = WS_O + h * 8 * 2048;
    u32x4 go = BLD(rw, hi * 2048 + lo * 16, sob + wave * 128, AUX_SC1);
    u32x4 gm = BLD(rw, hi * 2048, sob + 1024, AUX_SC1);
    asm volatile("" ::: "memory");
    f32x2 wa[4], wb[4];
#pragma unroll
    for (int k = 0; k < 4; ++k) { wa[k] = f32x2{lo16(woa[k]), hi16(woa[k])}; wb[k] = f32x2{lo16(wob[k]), hi16(wob[k])}; }
    asm volatile("" : "+v"(wa[0]), "+v"(wa[1]), "+v"(wa[2]), "+v"(wa[3]), "+v"(wb[0]), "+v"(wb[1]), "+v"(wb[2]), "+v"(wb[3]));
    SB();
    while (ANY(BAD(go) | BAD(gm))) {
      __builtin_amdgcn_s_sleep(CFG_SLEEP);
      go = BLD(rw, hi * 2048 + lo * 16, sob + wave * 128, AUX_SC1);
      gm = BLD(rw, hi * 2048, sob + 1024, AUX_SC1);
      asm volatile("" ::: "memory");
    }
    STAMP(S_opoll);
    const float mj = asf(gm[0]);
    const float M = xrow_max(fmaxf(mj, dpp<XOR8>(mj)));
    const float e = __builtin_amdgcn_exp2f(mj - M);
    const float L = hi8_sum(e * asf(gm[2]));
    const float rl = __builtin_amdgcn_rcpf(L);
    const float oa = hi8_sum(e * asf(go[0])) * rl, ob = hi8_sum(e * asf(go[2])) * rl;
    float y[8];
#pragma unroll
    for (int k = 0; k < 4; ++k) {
      const f32x2 t = __builtin_elementwise_fma(f32x2{ob, ob}, wb[k], f32x2{oa, oa} * wa[k]);
      y[2 * k] = t[0]; y[2 * k + 1] = t[1];
    }
#pragma unroll
    for (int k = 0; k < 8; ++k) { y[k] += dpp<XOR1>(y[k]); y[k] += dpp<XOR2>(y[k]); y[k] += dpp<XOR7>(y[k]); }
    if (lo == 0) {
      *(f32x4*)(l_y + wave * 64 + hi * 8) = f32x4{y[0], y[1], y[2], y[3]};
      *(f32x4*)(l_y + wave * 64 + hi * 8 + 4) = f32x4{y[4], y[5], y[6], y[7]};
    }
  }
  lds_barrier();
  STAMP(S_bar3);
  FLUSH(wave);
}

extern "C" void kernel_launch(void* const* d_in, const int* in_sizes, int n_in, void* d_out, int out_size, void* d_ws,
                              size_t ws_size, hipStream_t stream) {
  fused_kernel<<<NWG, NTHR, 0, stream>>>((const u16*)d_in[0], (const u16*)d_in[1], (const u16*)d_in[2], (const u16*)d_in[3],
                                         (const u16*)d_in[4], (const u16*)d_in[6], (const u16*)d_in[7], (const u16*)d_in[8],
                                         (const u16*)d_in[9], (u16*)d_out, (unsigned char*)d_ws);
}
```

```cpp
#include <hip/hip_runtime.h>
#include <stdint.h>

#define CFG_NS 62
#define CFG_D 2
#define CFG_TH0 -100
#define CFG_NB 8
#define CFG_GS0 30
#define CFG_GS1 10
#define CFG_TAU 436
#define CFG_ROFF 50
#define CFG_SLEEP 1
#define CFG_LK 3
#define CFG_TH -100
#define CFG_WS 16
#define CFG_VT 10
#define CFG_PF 1
#define CFG_GEN 0
#define CFG_MAP 0
#define CFG_FP 16
#define CFG_GT 0
#define CFG_HD 0
#define CFG_HG 0
#define CFG_FAKE 0
#define CFG_KA 1
#define CFG_TT 0
#define CFG_POOL 1
#define CFG_WP 0

typedef unsigned int u32;
typedef unsigned long long u64;
typedef unsigned short u16;
typedef u32 u32x4 __attribute__((ext_vector_type(4)));
typedef float f32x4 __attribute__((ext_vector_type(4)));
typedef float f32x2 __attribute__((ext_vector_type(2)));
typedef __bf16 bf16x2 __attribute__((ext_vector_type(2)));
typedef __amdgpu_buffer_rsrc_t rsrc_t;

constexpr int DM = 512, NH = 32, SK = 65536, DH = 128, FF = 1792;
constexpr int NWG = 256, NW = 8, NSV = 3, NTHR = (NW + NSV) * 64;
constexpr int CHUNK = SK / 8 * DH * 2;
constexpr int WS_GEN = 0;
constexpr int WS_CT = 8192;
constexpr int WS_B = 16384;
constexpr int WS_Q = WS_B + NWG * 512;
constexpr int WS_O = WS_Q + NWG * 2048;
constexpr int WS_Y = WS_O + NWG * 2048;
constexpr int WS_BYTES = WS_Y + NWG * 512;
constexpr int AUX_NT = 2, AUX_W = 2, AUX_SC1 = 16, AUX_ST = 16;

__device__ __forceinline__ rsrc_t mkrsrc(const void* p, int bytes) {
  return __builtin_amdgcn_make_buffer_rsrc((void*)p, 0, bytes, 0x00020000);
}
__device__ __forceinline__ float asf(u32 x) { return __builtin_bit_cast(float, x); }
__device__ __forceinline__ u32 asu(float x) { return __builtin_bit_cast(u32, x); }
__device__ __forceinline__ float lo16(u32 x) { return asf(x << 16); }
__device__ __forceinline__ float hi16(u32 x) { return asf(x & 0xffff0000u); }
__device__ __forceinline__ float rbf(float x) { __bf16 b = (__bf16)x; return (float)b; }
__device__ __forceinline__ u32 pk2(float a, float b) { return __builtin_bit_cast(u32, bf16x2{(__bf16)a, (__bf16)b}); }
__device__ __forceinline__ float dot2(u32 a, u32 b, float acc) {
  return __builtin_amdgcn_fdot2_f32_bf16(__builtin_bit_cast(bf16x2, a), __builtin_bit_cast(bf16x2, b), acc, false);
}
template <int CTRL> __device__ __forceinline__ float dpp(float x) {
  return __builtin_bit_cast(float, __builtin_amdgcn_mov_dpp(__builtin_bit_cast(int, x), CTRL, 0xf, 0xf, true));
}
constexpr int XOR1 = 0xB1, XOR2 = 0x4E, XOR7 = 0x141, XOR8 = 0x128, ROR4 = 0x124;
__device__ __forceinline__ float row16_sum(float x) {
  x += dpp<XOR8>(x); x += dpp<XOR7>(x); x += dpp<XOR2>(x); x += dpp<XOR1>(x); return x;
}
__device__ __forceinline__ float xrow_sum(float x) {
  auto s = __builtin_amdgcn_permlane16_swap(asu(x), asu(x), false, false);
  x = asf(s[0]) + asf(s[1]);
  auto t = __builtin_amdgcn_permlane32_swap(asu(x), asu(x), false, false);
  return asf(t[0]) + asf(t[1]);
}
__device__ __forceinline__ float xrow_max(float x) {
  auto s = __builtin_amdgcn_permlane16_swap(asu(x), asu(x), false, false);
  x = fmaxf(asf(s[0]), asf(s[1]));
  auto t = __builtin_amdgcn_permlane32_swap(asu(x), asu(x), false, false);
  return fmaxf(asf(t[0]), asf(t[1]));
}
__device__ __forceinline__ float hi8_sum(float x) { x += dpp<XOR8>(x); return xrow_sum(x); }
__device__ __forceinline__ float wave_sum(float x) { return xrow_sum(row16_sum(x)); }
__device__ __forceinline__ void lds_barrier() {
  asm volatile("s_waitcnt lgkmcnt(0)" ::: "memory");
  __builtin_amdgcn_s_barrier();
  asm volatile("" ::: "memory");
}
#define BLD(r, vo, so, aux) __builtin_amdgcn_raw_buffer_load_b128(r, vo, so, aux)
#define BST(v, r, vo, aux) __builtin_amdgcn_raw_buffer_store_b128(v, r, vo, 0, aux)
#define BAD(v) (((v)[1] ^ tlo) | ((v)[3] ^ thi))
#define ANY(x) (__builtin_amdgcn_ballot_w64((x) != 0) != 0)
#define SB() __builtin_amdgcn_sched_barrier(0)

#define STAMP(i) do { } while (0)
#define FLUSH(w) do { } while (0)

__device__ __forceinline__ f32x2 hi8_fold(const f32x2* a) {
  float b[4];
#pragma unroll
  for (int i = 0; i < 4; ++i) {
    const auto t = __builtin_amdgcn_permlane32_swap(asu(a[i >> 1][i & 1]), asu(a[2 + (i >> 1)][i & 1]), false, false);
    b[i] = asf(t[0]) + asf(t[1]);
  }
  f32x2 o;
#pragma unroll
  for (int i = 0; i < 2; ++i) {
    const auto t = __builtin_amdgcn_permlane16_swap(asu(b[i]), asu(b[2 + i]), false, false);
    const float v = asf(t[0]) + asf(t[1]);
    o[i] = v + dpp<XOR8>(v);
  }
  return o;
}
__device__ __forceinline__ float half_sum(float x) {
  const auto t = __builtin_amdgcn_permlane32_swap(asu(x), asu(x), false, false);
  return asf(t[0]) + asf(t[1]);
}

__global__ void __launch_bounds__(NTHR)
fused_kernel(const u16* x, const u16* K, const u16* V, const u16* wq, const u16* wo, const u16* w1, const u16* b1,
             const u16* w2, const u16* b2, u16* out, unsigned char* ws
) {
  constexpr int U = 4, NB = CFG_NB, NS = CFG_NS, D = CFG_D, FG = FF / NH, TAU = CFG_TAU;
  constexpr int TILE = U * 1024, NT = CHUNK / (TILE * NW), NGRP = (NT - NS) * NW;
  static_assert(NB >= D + 1 && NS >= NB + 2 * D && NS <= NT, "");
  __shared__ __attribute__((aligned(16))) float lds[24 * 1024];
  float* const l_y = lds;
  float* const l_acc = lds + 512;
  float* const l_h = l_acc + 8 * 132;
  u32* const l_q = (u32*)(l_h + 64);
  float* const l_s = l_h + 64 + 128;

  const int bid = blockIdx.x, tid = threadIdx.x, lane = tid & 63;
  STAMP(S_start);
  u32 pf = 0;
  if (CFG_PF) {
    u64 pc;
    int len;
    asm volatile("s_getpc_b64 %0\n.Lcode_from:\n\ts_mov_b32 %1, .Lcode_to-.Lcode_from" : "=s"(pc), "=s"(len));
    pf = __builtin_amdgcn_raw_buffer_load_b32(mkrsrc((const void*)pc, len), tid * 128, 0, 0);
    asm volatile("" ::: "memory");
    SB();
  }
  const int wg = CFG_MAP == 0 ? bid : CFG_MAP == 1 ? (bid & 31) * 8 + (bid >> 5) : CFG_MAP == 2 ? (bid & 7) * 32 + ((bid >> 3) & 3) * 8 + (bid >> 5)
    : (bid & 7) * 8 + (bid >> 6) * 64 + ((bid >> 3) & 7);
  const int wv = __builtin_amdgcn_readfirstlane(tid >> 6), wave = (wv - NSV) & 7;
  const int r = lane >> 4, c = lane & 15, hi = lane >> 3, lo = lane & 7;
  const int h = wg >> 3, s = wg & 7;
  const int vo = lane * 16;
  const rsrc_t rw = mkrsrc(ws, WS_BYTES);

  if (CFG_KA) asm volatile("" :: "s"(K), "s"(b2), "s"(out), "s"(ws));
  const rsrc_t rk = mkrsrc((const char*)K + (size_t)h * (8 * CHUNK), wv < NSV ? 0 : 8 * CHUNK);
  constexpr int RSTEP = NW * TILE, SSTEP = CHUNK, WSTEP = TILE;
  const int sb = s * SSTEP + wave * WSTEP;
  u32x4 tb[NB][U];
#define ISSUE(sl, rs_, so_) do { _Pragma("unroll") for (int u = 0; u < U; ++u) tb[sl][u] = BLD(rs_, vo + u * 1024, so_, AUX_NT); asm volatile("" ::: "memory"); } while (0)
#define TOUCH(sl) do { _Pragma("unroll") for (int u = 0; u < U; ++u) asm volatile("" : "+v"(tb[sl][u]) :: "memory"); } while (0)
#define ST(t) (sb + (t) * RSTEP)
#define NOW() ((u32)__builtin_amdgcn_s_memrealtime() * 4)
#define LDS_LD(p) ((u32)__builtin_amdgcn_readfirstlane(__hip_atomic_load(p, __ATOMIC_RELAXED, __HIP_MEMORY_SCOPE_WORKGROUP)))
  ISSUE(0, rk, ST(0));
  const u32 tbase = (u32)__builtin_amdgcn_s_memrealtime();
  STAMP(S_issued);
  if (tid < 6) l_q[66 + tid] = 0;
  if (tid == NSV * 64) l_q[72] = tbase;
  lds_barrier();
  SB();

  if (__builtin_expect(wv < NSV, 1)) {
    const rsrc_t rx = mkrsrc(x, DM * 2);
    const bool ffn = wv == 2;
    const rsrc_t ra = ffn ? mkrsrc(w1, DM * FF * 2) : mkrsrc(wq, DM * NH * DH * 2);
    const int pitch = ffn ? FF * 2 : NH * DH * 2;
    const int voa = (ffn && lo == 7) ? DM * FF * 2 : hi * 8 * pitch + lo * 16;
    const int soa = s * 64 * pitch + (ffn ? h * (FG * 2) : h * 256 + wv * 128);
    if (CFG_HD && ffn) { const u32 tg = tbase * 4 + CFG_HD + bid * 16; while ((int)(NOW() - tg) < 0) __builtin_amdgcn_s_sleep(32); }
    asm volatile("" ::: "memory");
    const u32x4 xs = BLD(rx, vo, 0, 0);
    const u32x4 xv = BLD(rx, hi * 16, s * 128, 0);
    u32x4 wa[8];
#pragma unroll
    for (int j = 0; j < 8; ++j) wa[j] = BLD(ra, voa, soa + j * pitch, AUX_W);
    const auto gen2 = __builtin_amdgcn_raw_buffer_load_b64(rw, 0, WS_GEN, CFG_GEN);
    u32 ct0 = 0;
    if (NGRP && s == 0 && tid < 8) ct0 = __hip_atomic_exchange((u32*)(ws + WS_CT + h * 256) + tid, 0u, __ATOMIC_RELAXED, __HIP_MEMORY_SCOPE_AGENT);
    SB();
    const u32 b1v = __builtin_amdgcn_raw_buffer_load_b32(mkrsrc(b1, ffn ? FF * 2 : 0), (lane & 31) < FG / 2 ? (lane & 31) * 4 : FF * 2, h * (FG * 2), 0);
    const u32 xo = __builtin_amdgcn_raw_buffer_load_b32(rx, lo * 4, h * 32, 0);
    const u32 bo = __builtin_amdgcn_raw_buffer_load_b32(mkrsrc(b2, DM * 2), lo * 4, h * 32, 0);
    asm volatile("" ::: "memory");
    SB();
    float xn[8];
    {
      float e[8];
#pragma unroll
      for (int k = 0; k < 4; ++k) { e[2 * k] = lo16(xs[k]); e[2 * k + 1] = hi16(xs[k]); }
      float sum = ((e[0] + e[1]) + (e[2] + e[3])) + ((e[4] + e[5]) + (e[6] + e[7]));
      const float mean = wave_sum(sum) * (1.0f / DM);
      float sq = 0.f;
#pragma unroll
      for (int k = 0; k < 8; ++k) { const float d = e[k] - mean; sq = fmaf(d, d, sq); }
      const float var = wave_sum(sq) * (1.0f / DM);
      const float mean_b = rbf(mean);
      const float sd = rbf(__builtin_amdgcn_sqrtf(rbf(rbf(var) + 1e-5f)));
      const float rstd = __builtin_amdgcn_rcpf(sd);
#pragma unroll
      for (int k = 0; k < 4; ++k) { xn[2 * k] = rbf(rbf(lo16(xv[k]) - mean_b) * rstd); xn[2 * k + 1] = rbf(rbf(hi16(xv[k]) - mean_b) * rstd); }
    }
    asm volatile("" : "+v"(xn[0]), "+v"(xn[1]), "+v"(xn[2]), "+v"(xn[3]), "+v"(xn[4]), "+v"(xn[5]), "+v"(xn[6]), "+v"(xn[7]));
    SB();
    STAMP(S_ln);
    f32x2 a[4] = {{0, 0}, {0, 0}, {0, 0}, {0, 0}};
#pragma unroll
    for (int j = 0; j < 8; ++j)
#pragma unroll
      for (int k = 0; k < 4; ++k) a[k] = __builtin_elementwise_fma(f32x2{xn[j], xn[j]}, f32x2{lo16(wa[j][k]), hi16(wa[j][k])}, a[k]);
    const f32x2 part = hi8_fold(a);
    const u64 gen = ((u64)(u32)__builtin_amdgcn_readfirstlane(gen2[1]) << 32) | (u32)__builtin_amdgcn_readfirstlane(gen2[0]);
    const u64 tag = gen * 6364136223846793005ull + 1442695040888963407ull;
    const u32 tlo = (u32)tag, thi = (u32)(tag >> 32);
    asm volatile("" :: "v"(ct0), "v"(pf));
    STAMP(S_part);
    const int vop = (lo * 4 + r) * 16;
    BST((u32x4{asu(part[0]), tlo, asu(part[1]), thi}), rw, ((lane & 8) || (CFG_FAKE & 2)) ? WS_BYTES : WS_Q + wg * 2048 + wv * 512 + vop, AUX_ST);
    STAMP(S_qpub);
    f32x2 g;
    {
      const int vog = (lane >> 5) * 2048 + (lane & 31) * 16, sog = WS_Q + h * 8 * 2048 + wv * 512;
      u32x4 gq[4];
      if (ffn) { if (CFG_HD) { const u32 tg = tbase * 4 + CFG_HD + CFG_HG + bid * 16; while ((int)(NOW() - tg) < 0) __builtin_amdgcn_s_sleep(32); } else __builtin_amdgcn_s_sleep(100); }
      else if (CFG_GT) { const u32 tg = tbase * 4 + CFG_GT; while ((int)(NOW() - tg) < 0) __builtin_amdgcn_s_sleep(2); }
      else __builtin_amdgcn_s_sleep(CFG_GS0);
      if (CFG_FP != 16) {
#pragma unroll
        for (int j = 0; j < 4; ++j) gq[j] = BLD(rw, vog, sog + j * 4096, CFG_FP);
        asm volatile("" : "+v"(gq[0]), "+v"(gq[1]), "+v"(gq[2]), "+v"(gq[3]) :: "memory");
      }
      while (CFG_FP == 16 || ANY(BAD(gq[0]) | BAD(gq[1]) | BAD(gq[2]) | BAD(gq[3]))) {
#pragma unroll
        for (int j = 0; j < 4; ++j) gq[j] = BLD(rw, (CFG_FAKE & 16) ? WS_BYTES : vog, sog + j * 4096, AUX_SC1);
        asm volatile("" : "+v"(gq[0]), "+v"(gq[1]), "+v"(gq[2]), "+v"(gq[3]) :: "memory");
        if (CFG_FAKE & 1) { for (int j = 0; j < 4; ++j) gq[j] &= 0x3c003c00u; break; }
        if (!ANY(BAD(gq[0]) | BAD(gq[1]) | BAD(gq[2]) | BAD(gq[3]))) break;
        __builtin_amdgcn_s_sleep(CFG_GS1);
      }
      g[0] = half_sum((asf(gq[0][0]) + asf(gq[1][0])) + (asf(gq[2][0]) + asf(gq[3][0])));
      g[1] = half_sum((asf(gq[0][2]) + asf(gq[1][2])) + (asf(gq[2][2]) + asf(gq[3][2])));
    }
    STAMP(S_gath);
    if (!ffn) {
      if (lane < 32) l_q[wv * 32 + lane] = pk2(g[0], g[1]);
      if (lane == 0) { l_q[64] = tlo; l_q[65] = thi; }
      asm volatile("s_waitcnt lgkmcnt(0)" ::: "memory");
      if (lane == 0) __hip_atomic_fetch_add(l_q + 66, 1u, __ATOMIC_RELAXED, __HIP_MEMORY_SCOPE_WORKGROUP);
      STAMP(S_qready);
      if (CFG_VT && wv == 1) {
        for (int i = 0; i < CFG_VT; ++i) __builtin_amdgcn_s_sleep(127);
        u32 t = __builtin_amdgcn_raw_buffer_load_b32(mkrsrc((const char*)V + (size_t)wg * CHUNK, CHUNK), (lane & 1) * (CHUNK - 4), 0, AUX_NT);
        if (CFG_TT) {
          t += __builtin_amdgcn_raw_buffer_load_b32(mkrsrc(wo, NH * DH * DM * 2), (lane & 1) * (DH - 1) * (DM * 2), h * DH * (DM * 2), AUX_NT);
          t += __builtin_amdgcn_raw_buffer_load_b32(mkrsrc(w2, FF * DM * 2), (lane & 1) * (FG - 1) * (DM * 2), h * FG * (DM * 2), AUX_NT);
          t += __builtin_amdgcn_raw_buffer_load_b32(mkrsrc(out, DM * 2), 0, 0, AUX_NT);
        }
        asm volatile("" :: "v"(t));
      }
      lds_barrier();
      STAMP(S_bar2);
      if (wv == 0) {
        f32x2 ml[8], ov[8];
#pragma unroll
        for (int w = 0; w < 8; ++w) { ml[w] = *(const f32x2*)(l_acc + w * 132 + 128); ov[w] = *(const f32x2*)(l_acc + w * 132 + lane * 2); }
        float Mg = ml[0][0], Lg = 0.f;
#pragma unroll
        for (int w = 1; w < 8; ++w) Mg = fmaxf(Mg, ml[w][0]);
        f32x2 o = {0, 0};
#pragma unroll
        for (int w = 0; w < 8; ++w) {
          const float f = __builtin_amdgcn_exp2f(ml[w][0] - Mg);
          Lg = fmaf(f, ml[w][1], Lg);
          o = __builtin_elementwise_fma(f32x2{f, f}, ov[w], o);
        }
        BST((u32x4{asu(o[0]), tlo, asu(o[1]), thi}), rw, WS_O + wg * 2048 + vo, AUX_ST);
        BST((u32x4{asu(Mg), tlo, asu(Lg), thi}), rw, lane == 0 ? WS_O + wg * 2048 + 1024 : WS_BYTES, AUX_ST);
        STAMP(S_opub);
      }
      lds_barrier();
      if (wv == 0) {
        const float* src = l_y + (lane >> 5) * 256 + (lane & 31) * 2;
        const f32x2 t = (*(const f32x2*)src + *(const f32x2*)(src + 64)) + (*(const f32x2*)(src + 128) + *(const f32x2*)(src + 192));
        BST((u32x4{asu(half_sum(t[0])), tlo, asu(half_sum(t[1])), thi}), rw, lane < 32 ? WS_Y + wg * 512 + vo : WS_BYTES, AUX_ST);
        STAMP(S_ypub);
      }
    } else {
      {
        float h0 = rbf(rbf(g[0]) + lo16(b1v)), h1 = rbf(rbf(g[1]) + hi16(b1v));
        h0 = h0 > 0.f ? h0 : 0.f; h1 = h1 > 0.f ? h1 : 0.f;
        if (lane < 32) *(f32x2*)(l_h + lane * 2) = f32x2{rbf(h0 * h0), rbf(h1 * h1)};
        asm volatile("s_waitcnt lgkmcnt(0)" ::: "memory");
        while (__hip_atomic_load(l_q + 67, __ATOMIC_RELAXED, __HIP_MEMORY_SCOPE_WORKGROUP) == 0) __builtin_amdgcn_s_sleep(4);
        asm volatile("" ::: "memory");
        const rsrc_t r2 = mkrsrc(w2, FF * DM * 2);
        u32x4 wb[7];
#pragma unroll
        for (int j = 0; j < 7; ++j) wb[j] = BLD(r2, hi * (DM * 2) + lo * 16, (h * FG + j * 8) * (DM * 2) + s * 128, AUX_W);
        asm volatile("" ::: "memory");
        lds_barrier();
        f32x2 y[4] = {{0, 0}, {0, 0}, {0, 0}, {0, 0}};
#pragma unroll
        for (int j = 0; j < 7; ++j) {
          const float hh = l_h[j * 8 + hi];
#pragma unroll
          for (int k = 0; k < 4; ++k) y[k] = __builtin_elementwise_fma(f32x2{hh, hh}, f32x2{lo16(wb[j][k]), hi16(wb[j][k])}, y[k]);
        }
        const f32x2 t = hi8_fold(y);
        BST((u32x4{asu(t[0]), tlo, asu(t[1]), thi}), rw, (lane & 8) ? WS_BYTES : WS_B + wg * 512 + vop, AUX_ST);
        STAMP(S_bpub);
      }
      const bool fin = s == (h & 7);
      const int voy = hi * 4096 + lo * 16, soy = (h >> 2) * 512 + (h & 3) * 128;
      lds_barrier();
      if (fin) {
        u32x4 pb[4], pc[4];
        while (true) {
#pragma unroll
          for (int j = 0; j < 4; ++j) pc[j] = BLD(rw, voy, WS_B + soy + j * 32768, AUX_SC1);
#pragma unroll
          for (int j = 0; j < 4; ++j) pb[j] = BLD(rw, voy, WS_Y + soy + j * 32768, AUX_SC1);
          asm volatile("" : "+v"(pc[0]), "+v"(pc[1]), "+v"(pc[2]), "+v"(pc[3]) :: "memory");
          if (!ANY(BAD(pc[0]) | BAD(pc[1]) | BAD(pc[2]) | BAD(pc[3]))) break;
          __builtin_amdgcn_s_sleep(CFG_SLEEP);
        }
        const float c0 = hi8_sum((asf(pc[0][0]) + asf(pc[1][0])) + (asf(pc[2][0]) + asf(pc[3][0])));
        const float c1 = hi8_sum((asf(pc[0][2]) + asf(pc[1][2])) + (asf(pc[2][2]) + asf(pc[3][2])));
        float B0 = rbf(rbf(c0) + lo16(bo)), B1 = rbf(rbf(c1) + hi16(bo));
        asm volatile("" : "+v"(B0), "+v"(B1), "+v"(pb[0]), "+v"(pb[1]), "+v"(pb[2]), "+v"(pb[3]));
        while (ANY(BAD(pb[0]) | BAD(pb[1]) | BAD(pb[2]) | BAD(pb[3]))) {
#pragma unroll
          for (int j = 0; j < 4; ++j) pb[j] = BLD(rw, voy, WS_Y + soy + j * 32768, AUX_SC1);
          asm volatile("" : "+v"(pb[0]), "+v"(pb[1]), "+v"(pb[2]), "+v"(pb[3]) :: "memory");
        }
        STAMP(S_ypoll);
        const float A0 = hi8_sum((asf(pb[0][0]) + asf(pb[1][0])) + (asf(pb[2][0]) + asf(pb[3][0])));
        const float A1 = hi8_sum((asf(pb[0][2]) + asf(pb[1][2])) + (asf(pb[2][2]) + asf(pb[3][2])));
        const u32 res = pk2(rbf(lo16(xo) + rbf(A0)) + B0, rbf(hi16(xo) + rbf(A1)) + B1);
        if (lane < 8) ((u32*)out)[h * 8 + lane] = res;
        if (h == 0 && lane == 8) __hip_atomic_store((u64*)(ws + WS_GEN), tag, __ATOMIC_RELAXED, __HIP_MEMORY_SCOPE_AGENT);
        STAMP(S_final);
      }
    }
    FLUSH(wv);
    return;
  }

  u32 due = (LDS_LD(l_q + 72) + CFG_ROFF) * 4 + TAU;
  due += TAU - 1 - (due + TAU - 1 - bid * TAU / NWG) % TAU + wave * TAU / CFG_WS;
#define PACE() do { while ((int)(NOW() - due) < 0) __builtin_amdgcn_s_sleep(1); due += TAU; } while (0)
#define SLIP(late_) do { if (lane == 0) __hip_atomic_fetch_max((int*)l_q + 71, ((late_) / TAU + 1) * TAU, __ATOMIC_RELAXED, __HIP_MEMORY_SCOPE_WORKGROUP); asm volatile("" ::: "memory"); } while (0)
#define ARRIVE(th_) do { const int off_ = LDS_LD(l_q + 71), late_ = (int)(NOW() - due) - (th_); if (late_ > off_) SLIP(late_); } while (0)
#define PACEL(th_) do { while ((int)(NOW() - due - LDS_LD(l_q + 71)) < 0) __builtin_amdgcn_s_sleep(1); due += TAU; } while (0)
  const rsrc_t rv = mkrsrc((const char*)V + (size_t)h * (8 * CHUNK), 8 * CHUNK);
  asm volatile("" ::: "memory");
  SB();
#pragma unroll
  for (int t = 1; t < NB; ++t) {
    PACE();
    if (t <= 5) STAMP(S_t0 + t - 1);
    ISSUE(t, rk, ST(t));
    SB();
  }
  while (__hip_atomic_load(l_q + 66, __ATOMIC_RELAXED, __HIP_MEMORY_SCOPE_WORKGROUP) != 2) __builtin_amdgcn_s_sleep(1);
  asm volatile("" ::: "memory");
  STAMP(S_flag);
  asm volatile("" :: "v"(pf));
  const u32x4 qp = *(const u32x4*)(l_q + c * 4);
  const u32 tlo = __builtin_amdgcn_readfirstlane(l_q[64]), thi = __builtin_amdgcn_readfirstlane(l_q[65]);
  const bool q0 = lane & 1, q1 = lane & 2;
  float* const ls = l_s + wave * (NS * 16) + r * 4 + (lane & 3);
  float m = -__builtin_inff(), l = 0.f, sc;
  f32x2 acc[4] = {{0, 0}, {0, 0}, {0, 0}, {0, 0}};
#define SCORE(sl) do { \
    float d[U]; \
    _Pragma("unroll") for (int u = 0; u < U; ++u) { \
      d[u] = dot2(tb[sl][u][0], qp[0], 0.f); d[u] = dot2(tb[sl][u][1], qp[1], d[u]); d[u] = dot2(tb[sl][u][2], qp[2], d[u]); d[u] = dot2(tb[sl][u][3], qp[3], d[u]); } \
    const float e0 = (q0 ? d[1] : d[0]) + dpp<XOR1>(q0 ? d[0] : d[1]), e1 = (q0 ? d[3] : d[2]) + dpp<XOR1>(q0 ? d[2] : d[3]); \
    float f = (q1 ? e1 : e0) + dpp<XOR2>(q1 ? e0 : e1); \
    f += dpp<XOR8>(f); f += dpp<ROR4>(f); \
    sc = rbf(rbf(f) * 0.08838834764831845f) * 1.4426950408889634f; \
  } while (0)
#define KTILE(sl, t) do { SCORE(sl); ls[(t) * 16] = sc; m = fmaxf(m, sc); asm volatile("" : "+v"(m) :: "memory"); } while (0)
#define ACCUM(sl) do { \
    const float p = __builtin_amdgcn_exp2f(sc - m); l += p; \
    const float pu[U] = {dpp<0x00>(p), dpp<0x55>(p), dpp<0xAA>(p), dpp<0xFF>(p)}; \
    _Pragma("unroll") for (int u = 0; u < U; ++u) \
      _Pragma("unroll") for (int k = 0; k < 4; ++k) acc[k] = __builtin_elementwise_fma(f32x2{pu[u], pu[u]}, f32x2{lo16(tb[sl][u][k]), hi16(tb[sl][u][k])}, acc[k]); \
    asm volatile("" : "+v"(acc[0]), "+v"(acc[1]), "+v"(acc[2]), "+v"(acc[3]), "+v"(l)); \
  } while (0)
#define VTILE(sl, t) do { sc = ls[(t) * 16]; ACCUM(sl); } while (0)
#define VTILE_RUN(sl) do { \
    float mn = fmaxf(sc, dpp<XOR1>(sc)); mn = fmaxf(mn, dpp<XOR2>(mn)); mn = fmaxf(mn, m); \
    const float alpha = __builtin_amdgcn_exp2f(m - mn); \
    m = mn; l *= alpha; \
    _Pragma("unroll") for (int k = 0; k < 4; ++k) acc[k] *= f32x2{alpha, alpha}; \
    ACCUM(sl); \
  } while (0)
#define BUF(i) (((i) - NB) % D)
  constexpr int CH = (NB + D - 2) / D, K1 = NB + D + (NS - NB - D) / D * D, V1 = D + (NS - D - 2) / D * D;
#pragma unroll
  for (int k = 0; k <= D; ++k) {
#pragma unroll
    for (int t = k ? 1 + (k - 1) * CH : 0; t < 1 + k * CH && t < NB; ++t) KTILE(t, t);
    SB();
    if (k < D) { ARRIVE(CFG_TH0); PACEL(CFG_TH0); ISSUE(k, rk, ST(NB + k)); }
  }
  STAMP(S_t5);
  for (int i = NB + D; i < K1; i += D) {
#pragma unroll
    for (int ph = 0; ph < D; ++ph) {
      TOUCH(ph); ARRIVE(CFG_TH);
      KTILE(ph, i + ph - D);
      SB();
      PACEL(CFG_TH);
      ISSUE(ph, rk, ST(i + ph));
    }
  }
#pragma unroll
  for (int i = K1; i < NS + D; ++i) {
    TOUCH(BUF(i)); ARRIVE(CFG_TH);
    KTILE(BUF(i), i - D);
    SB();
    PACEL(CFG_TH);
    if (i < NS) ISSUE(BUF(i), rk, ST(i)); else ISSUE(BUF(i), rv, ST(i - NS));
  }
  STAMP(S_kend);
  m = fmaxf(m, dpp<XOR1>(m)); m = fmaxf(m, dpp<XOR2>(m)); m = xrow_max(m);
  for (int i = D; i < V1; i += D) {
#pragma unroll
    for (int ph = 0; ph < D; ++ph) {
      TOUCH(BUF(NS + ph)); ARRIVE(CFG_TH);
      VTILE(BUF(NS + ph), i + ph - D);
      SB();
      PACEL(CFG_TH);
      ISSUE(BUF(NS + ph), rv, ST(i + ph));
    }
  }
  STAMP(S_statend);
  constexpr int KS = BUF(2 * NS - 2), VS = BUF(2 * NS - 1);
  {
    int cvo = lane < 8 ? WS_CT + h * 256 + lane * 4 : WS_BYTES;
    int vic = s;
    asm volatile("" : "+v"(cvo));
    int tk = 0;
#define DRAW() do { tk = __builtin_amdgcn_raw_ptr_buffer_atomic_add_i32((int)(lane == vic), rw, cvo, 0, 0); asm volatile("" ::: "memory"); } while (0)
#pragma unroll
    for (int i = V1; i < NS; ++i) {
      TOUCH(BUF(NS + i)); ARRIVE(CFG_TH);
      VTILE(BUF(NS + i), i - D);
      SB();
      PACEL(CFG_TH);
      ISSUE(BUF(NS + i), rv, ST(i));
      if (NGRP && i == NS - 2) DRAW();
    }
#pragma unroll
    for (int i = NS - D; i < NS - 1; ++i) {
      TOUCH(BUF(NS + i));
      VTILE(BUF(NS + i), i);
    }
    due += LDS_LD(l_q + 71);
    sc = ls[(NS - 1) * 16];
    SB();
    while (NGRP) {
      u32 g = __builtin_amdgcn_readlane(tk, vic);
      while (g >= (u32)NGRP) {
        const u32 m8 = (u32)__builtin_amdgcn_ballot_w64((u32)tk < (u32)NGRP) & 0xffu;
        if (!m8) break;
        const int rt = (s + wave) & 7;
        vic = (rt + __builtin_ctz((m8 | m8 << 8) >> rt)) & 7;
        DRAW();
        g = __builtin_amdgcn_readlane(tk, vic);
      }
      if (g >= (u32)NGRP) break;
      const int o = vic * SSTEP + (NS + (g >> 3)) * RSTEP + (g & 7) * WSTEP;
      if (vic == s) PACE();
      ISSUE(KS, rk, o);
      DRAW();
      TOUCH(VS);
      VTILE_RUN(VS);
      SB();
      if (vic == s) PACE();
      ISSUE(VS, rv, o);
      TOUCH(KS);
      SCORE(KS);
      asm volatile("" : "+v"(sc));
      SB();
    }
  }
  if (CFG_WP) PACE();
  const rsrc_t ro = mkrsrc(wo, NH * DH * DM * 2);
  const u32x4 woa = BLD(ro, lo * 2 * (DM * 2) + hi * 16, (h * DH + wave * 16) * (DM * 2) + s * 128, AUX_W);
  const u32x4 wob = BLD(ro, lo * 2 * (DM * 2) + hi * 16, (h * DH + wave * 16 + 1) * (DM * 2) + s * 128, AUX_W);
  asm volatile("" ::: "memory");
  if (lane == 0) l_q[67] = 1;
  SB();
  VTILE_RUN(VS);
  l += dpp<XOR1>(l); l += dpp<XOR2>(l);
  STAMP(S_loopend);

  {
    const float mw = xrow_max(m);
    const float f = __builtin_amdgcn_exp2f(m - mw);
    l = xrow_sum(l * f);
    float a[8];
#pragma unroll
    for (int k = 0; k < 4; ++k) { a[2 * k] = xrow_sum(acc[k][0] * f); a[2 * k + 1] = xrow_sum(acc[k][1] * f); }
    if (r == 0) {
      *(f32x4*)(l_acc + wave * 132 + c * 8) = f32x4{a[0], a[1], a[2], a[3]};
      *(f32x4*)(l_acc + wave * 132 + c * 8 + 4) = f32x4{a[4], a[5], a[6], a[7]};
      if (c == 0) *(f32x2*)(l_acc + wave * 132 + 128) = f32x2{mw, l};
    }
  }
  lds_barrier();
  {
    const int sob = WS_O + h * 8 * 2048;
    u32x4 go = BLD(rw, hi * 2048 + lo * 16, sob + wave * 128, AUX_SC1);
    u32x4 gm = BLD(rw, hi * 2048, sob + 1024, AUX_SC1);
    asm volatile("" ::: "memory");
    f32x2 wa[4], wb[4];
#pragma unroll
    for (int k = 0; k < 4; ++k) { wa[k] = f32x2{lo16(woa[k]), hi16(woa[k])}; wb[k] = f32x2{lo16(wob[k]), hi16(wob[k])}; }
    asm volatile("" : "+v"(wa[0]), "+v"(wa[1]), "+v"(wa[2]), "+v"(wa[3]), "+v"(wb[0]), "+v"(wb[1]), "+v"(wb[2]), "+v"(wb[3]));
    SB();
    while (ANY(BAD(go) | BAD(gm))) {
      __builtin_amdgcn_s_sleep(CFG_SLEEP);
      go = BLD(rw, hi * 2048 + lo * 16, sob + wave * 128, AUX_SC1);
      gm = BLD(rw, hi * 2048, sob + 1024, AUX_SC1);
      asm volatile("" ::: "memory");
    }
    STAMP(S_opoll);
    const float mj = asf(gm[0]);
    const float M = xrow_max(fmaxf(mj, dpp<XOR8>(mj)));
    const float e = __builtin_amdgcn_exp2f(mj - M);
    const float L = hi8_sum(e * asf(gm[2]));
    const float rl = __builtin_amdgcn_rcpf(L);
    const float oa = hi8_sum(e * asf(go[0])) * rl, ob = hi8_sum(e * asf(go[2])) * rl;
    float y[8];
#pragma unroll
    for (int k = 0; k < 4; ++k) {
      const f32x2 t = __builtin_elementwise_fma(f32x2{ob, ob}, wb[k], f32x2{oa, oa} * wa[k]);
      y[2 * k] = t[0]; y[2 * k + 1] = t[1];
    }
#pragma unroll
    for (int k = 0; k < 8; ++k) { y[k] += dpp<XOR1>(y[k]); y[k] += dpp<XOR2>(y[k]); y[k] += dpp<XOR7>(y[k]); }
    if (lo == 0) {
      *(f32x4*)(l_y + wave * 64 + hi * 8) = f32x4{y[0], y[1], y[2], y[3]};
      *(f32x4*)(l_y + wave * 64 + hi * 8 + 4) = f32x4{y[4], y[5], y[6], y[7]};
    }
  }
  lds_barrier();
  STAMP(S_bar3);
  FLUSH(wave);
  asm volatile(".Lcode_to:");
}

extern "C" void kernel_launch(void* const* d_in, const int* in_sizes, int n_in, void* d_out, int out_size, void* d_ws,
                              size_t ws_size, hipStream_t stream) {
  fused_kernel<<<NWG, NTHR, 0, stream>>>((const u16*)d_in[0], (const u16*)d_in[1], (const u16*)d_in[2], (const u16*)d_in[3],
                                         (const u16*)d_in[4], (const u16*)d_in[6], (const u16*)d_in[7], (const u16*)d_in[8],
                                         (const u16*)d_in[9], (u16*)d_out, (unsigned char*)d_ws);
}
```

```cpp
#include <hip/hip_runtime.h>
#include <stdint.h>

#define CFG_WD 0
#define CFG_SP 0
#define CFG_BS 12
#define CFG_VX 0
#define CFG_PF0 0
#define CFG_PN 0
#define CFG_SL 1
#define CFG_CS 0
#define CFG_CT 128
#define CFG_AQ 2
#define CFG_GSF 100
#define CFG_CB 32
#define CFG_AL 16
#define CFG_AS 16
#define CFG_AX 0
#define CFG_AP 0
#define CFG_T0 100
#define CFG_N0 0
#define CFG_TT -300
#define CFG_TN 8
#define CFG_PS1 12
#define CFG_PS2 10
#define CFG_GS0 30
#define CFG_GS1 10
#define CFG_TAU 422
#define CFG_ROFF 80
#define CFG_TH 100
#define CFG_FX 0
#define CFG_ES 40
#define CFG_E0 1
#define CFG_VG 0
#define CFG_PF 0
#define CFG_BAR 0
#define CFG_WT 0
#define CFG_WF 0
#define CFG_TP 1300
#define CFG_TQ 1700
#define CFG_NP 1
#define CFG_NQ 2
#define CFG_WU 7
#define CFG_WP 0
#define CFG_BO 12
#define CFG_W2 600
#define CFG_FI 0
#define CFG_TS 2

typedef unsigned int u32;
typedef unsigned long long u64;
typedef unsigned short u16;
typedef u32 u32x2 __attribute__((ext_vector_type(2)));
typedef u32 u32x4 __attribute__((ext_vector_type(4)));
typedef u32 u32x32 __attribute__((ext_vector_type(32)));
typedef float f32x4 __attribute__((ext_vector_type(4)));
typedef float f32x16 __attribute__((ext_vector_type(16)));
typedef u32 u32x8 __attribute__((ext_vector_type(8)));
typedef float f32x2 __attribute__((ext_vector_type(2)));
typedef __bf16 bf16x2 __attribute__((ext_vector_type(2)));
typedef __amdgpu_buffer_rsrc_t rsrc_t;

constexpr int DM = 512, NH = 32, SK = 65536, DH = 128, FF = 1792;
constexpr int NWG = 256, NW = 8, NSV = 3, NTHR = (NW + NSV) * 64;
constexpr int CHUNK = SK / 8 * DH * 2;
constexpr int WS_GEN = 0;
constexpr int WS_CT = 8192;
constexpr int WS_B = 16384;
constexpr int WS_Q = WS_B + NWG * 512;
constexpr int WS_O = WS_Q + NWG * 2048;
constexpr int WS_Y = WS_O + NWG * 2048;
constexpr int WS_P = WS_Y + NWG * 512;
constexpr int WS_BYTES = WS_P + NWG * 128;
constexpr int AUX_NT = 2, AUX_W = 2, AUX_SC1 = CFG_AL, AUX_ST = CFG_AS;

__device__ __forceinline__ rsrc_t mkrsrc(const void* p, int bytes) {
  return __builtin_amdgcn_make_buffer_rsrc((void*)p, 0, bytes, 0x00020000);
}
__device__ __forceinline__ float asf(u32 x) { return __builtin_bit_cast(float, x); }
__device__ __forceinline__ u32 asu(float x) { return __builtin_bit_cast(u32, x); }
__device__ __forceinline__ float lo16(u32 x) { return asf(x << 16); }
__device__ __forceinline__ float hi16(u32 x) { return asf(x & 0xffff0000u); }
__device__ __forceinline__ float rbf(float x) { __bf16 b = (__bf16)x; return (float)b; }
__device__ __forceinline__ u32 pk2(float a, float b) { return __builtin_bit_cast(u32, bf16x2{(__bf16)a, (__bf16)b}); }
__device__ __forceinline__ float dot2(u32 a, u32 b, float acc) {
  return __builtin_amdgcn_fdot2_f32_bf16(__builtin_bit_cast(bf16x2, a), __builtin_bit_cast(bf16x2, b), acc, false);
}
template <int CTRL> __device__ __forceinline__ float dpp(float x) {
  return __builtin_bit_cast(float, __builtin_amdgcn_mov_dpp(__builtin_bit_cast(int, x), CTRL, 0xf, 0xf, true));
}
constexpr int XOR1 = 0xB1, XOR2 = 0x4E, XOR7 = 0x141, XOR8 = 0x128, ROR4 = 0x124;
__device__ __forceinline__ float row16_sum(float x) {
  x += dpp<XOR8>(x); x += dpp<XOR7>(x); x += dpp<XOR2>(x); x += dpp<XOR1>(x); return x;
}
__device__ __forceinline__ float xrow_sum(float x) {
  auto s = __builtin_amdgcn_permlane16_swap(asu(x), asu(x), false, false);
  x = asf(s[0]) + asf(s[1]);
  auto t = __builtin_amdgcn_permlane32_swap(asu(x), asu(x), false, false);
  return asf(t[0]) + asf(t[1]);
}
__device__ __forceinline__ float xrow_max(float x) {
  auto s = __builtin_amdgcn_permlane16_swap(asu(x), asu(x), false, false);
  x = fmaxf(asf(s[0]), asf(s[1]));
  auto t = __builtin_amdgcn_permlane32_swap(asu(x), asu(x), false, false);
  return fmaxf(asf(t[0]), asf(t[1]));
}
__device__ __forceinline__ float hi8_sum(float x) { x += dpp<XOR8>(x); return xrow_sum(x); }
__device__ __forceinline__ float wave_sum(float x) { return xrow_sum(row16_sum(x)); }
__device__ __forceinline__ void lds_barrier() {
  asm volatile("s_waitcnt lgkmcnt(0)" ::: "memory");
  __builtin_amdgcn_s_barrier();
  asm volatile("" ::: "memory");
}
#define BLD(r, vo, so, aux) __builtin_amdgcn_raw_buffer_load_b128(r, vo, so, aux)
#define BST(v, r, vo, aux) __builtin_amdgcn_raw_buffer_store_b128(v, r, vo, 0, aux)
#define BAD(v) (((v)[1] ^ tlo) | ((v)[3] ^ thi))
#define ANY(x) (__builtin_amdgcn_ballot_w64((x) != 0) != 0)
#define SB() __builtin_amdgcn_sched_barrier(0)
#define STR_(x) #x
#define STR(x) STR_(x)
#define PV(n, k) "v[40+4*(" #n ")+" #k "]"
#define PLD(n, vo, so) "buffer_load_dwordx4 v[40+4*(" #n "):43+4*(" #n ")], %[" #vo "], %[rs], %[" #so "] offen sc1\n"
#define PCK0(n) "v_xor_b32 %[t], %[tl], " PV(n, 1) "\nv_bitop3_b32 %[t], " PV(n, 3) ", %[th], %[t] bitop3:0xbe\n"
#define PCK(n) "v_bitop3_b32 %[t], " PV(n, 1) ", %[tl], %[t] bitop3:0xbe\nv_bitop3_b32 %[t], " PV(n, 3) ", %[th], %[t] bitop3:0xbe\n"
#define PDROP "v_cmp_eq_u32 vcc, 0, %[t]\ns_andn2_b64 exec, exec, vcc\ns_cbranch_execz .Lgot%=\n"
#define PEND "s_branch .Lpoll%=\n.Lgot%=:\ns_mov_b64 exec, -1\ns_nop 1"

__device__ __forceinline__ f32x2 xrow_fold(const f32x2* a) {
  float b[4];
#pragma unroll
  for (int i = 0; i < 4; ++i) {
    const auto t = __builtin_amdgcn_permlane32_swap(asu(a[i >> 1][i & 1]), asu(a[2 + (i >> 1)][i & 1]), false, false);
    b[i] = asf(t[0]) + asf(t[1]);
  }
  f32x2 o;
#pragma unroll
  for (int i = 0; i < 2; ++i) {
    const auto t = __builtin_amdgcn_permlane16_swap(asu(b[i]), asu(b[2 + i]), false, false);
    o[i] = asf(t[0]) + asf(t[1]);
  }
  return o;
}
__device__ __forceinline__ f32x2 hi8_fold(const f32x2* a) {
  const f32x2 o = xrow_fold(a);
  return f32x2{o[0] + dpp<XOR8>(o[0]), o[1] + dpp<XOR8>(o[1])};
}
__device__ __forceinline__ void hi8_merge(float& m, float& l, float& o0, float& o1) {
  const float M = xrow_max(fmaxf(m, dpp<XOR8>(m))), e = __builtin_amdgcn_exp2f(m - M);
  float a[3] = {e * l, e * o0, e * o1};
#pragma unroll
  for (int i = 0; i < 3; ++i) a[i] += dpp<XOR8>(a[i]);
  SB();
#pragma unroll
  for (int i = 0; i < 3; ++i) { const auto t = __builtin_amdgcn_permlane16_swap(asu(a[i]), asu(a[i]), false, false); a[i] = asf(t[0]) + asf(t[1]); }
  SB();
#pragma unroll
  for (int i = 0; i < 3; ++i) { const auto t = __builtin_amdgcn_permlane32_swap(asu(a[i]), asu(a[i]), false, false); a[i] = asf(t[0]) + asf(t[1]); }
  m = M; l = a[0]; o0 = a[1]; o1 = a[2];
}
__device__ __forceinline__ float half_sum(float x) {
  const auto t = __builtin_amdgcn_permlane32_swap(asu(x), asu(x), false, false);
  return asf(t[0]) + asf(t[1]);
}

#define B(i) (1u << (i))
struct Lay { u32 a[8], w[3], u[4], um, r[6], s[3]; bool ok; };
constexpr bool indep(u32* basis, u32 v) {
  for (int b = 31; b >= 0; --b) {
    if (!(v >> b & 1)) continue;
    if (!basis[b]) { basis[b] = v; return true; }
    v ^= basis[b];
  }
  return false;
}
#define CFG_CA B(21), B(26) ^ B(19) ^ B(14), B(27) ^ B(17), B(13), B(24) ^ B(18) ^ B(17) ^ B(14), B(25) ^ B(19) ^ B(18), B(17), B(28)
#define CFG_CW B(8), B(9), B(10), B(11), B(12), B(16) ^ B(15), B(14)
constexpr Lay make_lay() {
  constexpr u32 cols[15] = {CFG_CA, CFG_CW};
  Lay y = {};
  u32 basis[32] = {}, hb[32] = {};
  int n = 0, nr = 0, ns = 0;
  for (int i = 0; i < 15; ++i) {
    const u32 c = cols[i];
    if (i < 8) y.a[i] = c; else if (i < 12) { y.u[i - 8] = c; if (!CFG_VX) y.um |= c; } else y.w[i - 12] = c;
    n += !(c & 255) && c < B(i < 8 ? 29 : 24) && indep(basis, c);
  }
  for (int b = 8; b < 24 && nr < 6; ++b) if (!(y.um >> b & 1) && indep(basis, B(b))) y.r[nr++] = b;
  for (int k = 24; k < 29; ++k) {
    u32 v = 0;
    for (int i = 0; i < 8; ++i) v |= (y.a[i] >> k & 1) << i;
    indep(hb, v);
  }
  for (int i = 0; i < 8 && ns < 3; ++i) if (indep(hb, B(i))) y.s[ns++] = i;
  y.ok = n == 15 && nr == 6 && ns == 3;
  return y;
}
constexpr Lay LAY = make_lay();
__device__ __forceinline__ u32 row_off(u32 row) {
  u32 o = 0;
#pragma unroll
  for (int i = 0; i < 6; ++i) o |= (row >> i & 1) << LAY.r[i];
  return o;
}
__device__ __forceinline__ u32 wave_off(u32 w) { return (w & 1 ? LAY.w[0] : 0) ^ (w & 2 ? LAY.w[1] : 0) ^ (w & 4 ? LAY.w[2] : 0); }

__global__ void __launch_bounds__(NTHR)
fused_kernel(const u16* x, const u16* K, const u16* V, const u16* wq, const u16* wo, const u16* w1, const u16* b1,
             const u16* w2, const u16* b2, u16* out, unsigned char* ws) {
  constexpr int U = 4, NB = 8, NT = 64, NS = NT, D = 2, FG = FF / NH, TAU = CFG_TAU, HEAD = SK * DH * 2;
  __shared__ __attribute__((aligned(16))) float lds[24 * 1024];
  float* const l_y = lds;
  float* const l_acc = lds + 512;
  float* const l_h = l_acc + 8 * 132;
  u32* const l_q = (u32*)(l_h + 64);
  float* const l_s = l_h + 64 + 128;

  const int bid = blockIdx.x, tid = threadIdx.x, lane = tid & 63;
  u32 pf;
  {
    u64 pc;
    int len;
    asm volatile("s_getpc_b64 %0\n.Lcode_from:\n\ts_mov_b32 %1, .Lcode_to-.Lcode_from" : "=s"(pc), "=s"(len));
    pf = __builtin_amdgcn_raw_buffer_load_b32(mkrsrc((const void*)pc, len), tid * 128, 0, CFG_AP);
    asm volatile("" ::: "memory");
    SB();
  }
  u32 ab = 0;
#pragma unroll
  for (int i = 0; i < 8; ++i) ab ^= bid >> i & 1 ? LAY.a[i] : 0;
  const int h = ab >> 24, s = (bid >> LAY.s[0] & 1) | (bid >> LAY.s[1] & 1) << 1 | (bid >> LAY.s[2] & 1) << 2, wg = h * 8 + s;
  ab &= HEAD - 1;
  const int wv = __builtin_amdgcn_readfirstlane(tid >> 6), wave = (wv - NSV) & 7;
  const int r = lane >> 4, c = lane & 15, hi = lane >> 3, lo = lane & 7;
  const int vo = lane * 16;
  const u32 ph0 = bid * TAU / NWG;
  const rsrc_t rw = mkrsrc(ws, WS_BYTES);
  const u32x4 rws = {(u32)(uintptr_t)ws, (u32)((uintptr_t)ws >> 32) & 0xffffu, WS_BYTES, 0x00020000};

  const u32 sx = ab ^ wave_off(wave), sb = sx & ~LAY.um;
  int vu[U];
#pragma unroll
  for (int u = 0; u < U; ++u) vu[u] = c * 16 + ((r & 1 ? LAY.u[0] : 0) ^ (r & 2 ? LAY.u[1] : 0) ^ (u & 1 ? LAY.u[2] : 0) ^ (u & 2 ? LAY.u[3] : 0) ^ (sx & LAY.um));
  const u32 so0 = sb ^ row_off(NS - 1), kb = wv < NSV ? 0 : HEAD, ho = h * HEAD;
  asm volatile("" :: "s"(so0), "s"(kb), "s"(ho), "v"(vu[0]));
  SB();
  const rsrc_t rk = mkrsrc((const char*)K + ho, kb);
  u32x4 tb[NB][U];
#define ISSUE(sl, rs_, so_) do { _Pragma("unroll") for (int u = 0; u < U; ++u) tb[sl][u] = CFG_VX ? BLD(rs_, vu[u] ^ (so_), 0, AUX_NT) : BLD(rs_, vu[u], so_, AUX_NT); asm volatile("" ::: "memory"); } while (0)
#define TOUCH(sl) do { _Pragma("unroll") for (int u = 0; u < U; ++u) asm volatile("" : "+v"(tb[sl][u]) :: "memory"); } while (0)
#define NOW() ((u32)__builtin_amdgcn_s_memrealtime() * 4)
#define TR(k, j) do { } while (0)
#define TRV(k, j, v) do { } while (0)
#define LDS_LD(p) ((u32)__builtin_amdgcn_readfirstlane(__hip_atomic_load(p, __ATOMIC_RELAXED, __HIP_MEMORY_SCOPE_WORKGROUP)))
  ISSUE(0, rk, so0);  asm volatile("" :: "s"(b2), "s"(out), "s"(ws));
  const u32 tbase = (u32)__builtin_amdgcn_s_memrealtime();
  if (tid < 8 && tid != 4 && tid != 6) l_q[66 + tid] = 0;
  if (tid == NSV * 64) { l_q[72] = tbase; l_q[70] = tbase * 4; }
  lds_barrier();
  SB();

  if (__builtin_expect(wv < NSV, 1)) {
    const rsrc_t rx = mkrsrc(x, DM * 2);
    const bool ffn = wv == 2;
    constexpr int CB = CFG_CB, LP = CB / 8, RW = 512 / LP, NP = DM / RW;
    const int cg = CB == 64 ? 0 : CB == 32 ? s >> 1 & 1 : s >> 1, rp = CB == 64 ? s : CB == 32 ? (s & 1) | (s >> 2) << 1 : s & 1;
    const int c0 = ffn ? cg * CB : (cg * 2 + wv) * CB, cl = lane & (LP - 1);
    const rsrc_t ra = ffn ? mkrsrc(w1, DM * FF * 2) : mkrsrc(wq, DM * NH * DH * 2);
    const int pitch = ffn ? FF * 2 : NH * DH * 2;
    const int voa = (ffn && c0 + cl * 8 >= FG) ? DM * FF * 2 : lane / LP * 8 * pitch + cl * 16;
    const int soa = rp * RW * pitch + (ffn ? h * FG : h * DH) * 2 + c0 * 2;
    asm volatile("" ::: "memory");
    const u32x4 xs = BLD(rx, vo, 0, CFG_AX);
    const u32x4 xv = BLD(rx, lane / LP * 16, rp * RW * 2, CFG_AX);
    u32x4 wa[8];
#pragma unroll
    for (int j = 0; j < 8; ++j) wa[j] = BLD(ra, voa, soa + j * pitch, CFG_AQ);
    const auto gen2 = __builtin_amdgcn_raw_buffer_load_b64(rw, 0, WS_GEN, 0);
    SB();
    const u32 b1v = __builtin_amdgcn_raw_buffer_load_b32(mkrsrc(b1, ffn ? FF * 2 : 0), (lane & 31) < FG / 2 ? (lane & 31) * 4 : FF * 2, h * (FG * 2), 0);
    const u32 xo = __builtin_amdgcn_raw_buffer_load_b32(rx, lo * 4, h * 32, 0);
    const u32 bo = __builtin_amdgcn_raw_buffer_load_b32(mkrsrc(b2 + h * 16, 32), lo * 4, 0, 0);
    u32 wu = 0;
    if (ffn) {
      if (CFG_WU & 1) wu |= __builtin_amdgcn_raw_buffer_load_b32(mkrsrc(wo, NH * DH * DM * 2), 0, h * DH * (DM * 2) + s * 128, AUX_NT);
      if (CFG_WU & 2) wu |= __builtin_amdgcn_raw_buffer_load_b32(mkrsrc(w2, FF * DM * 2), 0, h * FG * (DM * 2) + s * 128, AUX_NT);
      if (CFG_WU & 4) wu |= __builtin_amdgcn_raw_buffer_load_b32(mkrsrc(out + h * 16, 32), 0, 0, AUX_SC1);
      if (CFG_WU & 8) wu |= __builtin_amdgcn_raw_buffer_load_b32(mkrsrc((const char*)V + ho, HEAD), (lane & 3) << 22, ab, AUX_NT);
      if (CFG_WU & 16) wu |= __builtin_amdgcn_raw_buffer_load_b32(mkrsrc((const char*)K + ho, HEAD), (lane & 3) << 22, ab, AUX_NT);
    }
    asm volatile("" ::: "memory");
    SB();
    float xn[8];
    {
      float e[8];
#pragma unroll
      for (int k = 0; k < 4; ++k) { e[2 * k] = lo16(xs[k]); e[2 * k + 1] = hi16(xs[k]); }
      float sum = ((e[0] + e[1]) + (e[2] + e[3])) + ((e[4] + e[5]) + (e[6] + e[7]));
      const float mean = wave_sum(sum) * (1.0f / DM);
      float sq = 0.f;
#pragma unroll
      for (int k = 0; k < 8; ++k) { const float d = e[k] - mean; sq = fmaf(d, d, sq); }
      const float var = wave_sum(sq) * (1.0f / DM);
      const float mean_b = rbf(mean);
      const float sd = rbf(__builtin_amdgcn_sqrtf(rbf(rbf(var) + 1e-5f)));
      const float rstd = __builtin_amdgcn_rcpf(sd);
#pragma unroll
      for (int k = 0; k < 4; ++k) { xn[2 * k] = rbf(rbf(lo16(xv[k]) - mean_b) * rstd); xn[2 * k + 1] = rbf(rbf(hi16(xv[k]) - mean_b) * rstd); }
    }
    asm volatile("" : "+v"(xn[0]), "+v"(xn[1]), "+v"(xn[2]), "+v"(xn[3]), "+v"(xn[4]), "+v"(xn[5]), "+v"(xn[6]), "+v"(xn[7]));
    SB();
    TR(1, 142);
    f32x2 a[4] = {{0, 0}, {0, 0}, {0, 0}, {0, 0}};
#pragma unroll
    for (int j = 0; j < 8; ++j)
#pragma unroll
      for (int k = 0; k < 4; ++k) a[k] = __builtin_elementwise_fma(f32x2{xn[j], xn[j]}, f32x2{lo16(wa[j][k]), hi16(wa[j][k])}, a[k]);
    f32x2 part = hi8_fold(a);
    if (LP < 8) part += f32x2{dpp<ROR4>(part[0]), dpp<ROR4>(part[1])};
    if (LP < 4) part += f32x2{dpp<XOR2>(part[0]), dpp<XOR2>(part[1])};
    const u64 gen = ((u64)(u32)__builtin_amdgcn_readfirstlane(gen2[1]) << 32) | (u32)__builtin_amdgcn_readfirstlane(gen2[0]);
    const u64 tag = gen * 6364136223846793005ull + 1442695040888963407ull;
    const u32 tlo = (u32)tag, thi = (u32)(tag >> 32);
    asm volatile("" :: "v"(pf));
    const int vop = (lo * 4 + r) * 16;
    BST((u32x4{asu(part[0]), tlo, asu(part[1]), thi}), rw, c >= LP ? WS_BYTES : WS_Q + (h * 3 + (ffn ? 2 : c0 >> 6)) * (NP * 512) + rp * 512 + (c0 & 63) * 8 + vop, AUX_ST);
    TR(1, 141);
    f32x2 g;
    {
      const int sog = WS_Q + (h * 3 + wv) * (NP * 512);
      u32x4 gq[NP / 2];
      if (ffn) __builtin_amdgcn_s_sleep(CFG_GSF); else __builtin_amdgcn_s_sleep(CFG_GS0);
      while (true) {
        u32 bad = 0;
#pragma unroll
        for (int j = 0; j < NP / 2; ++j) gq[j] = BLD(rw, vo, sog + j * 1024, AUX_SC1);
#pragma unroll
        for (int j = 0; j < NP / 2; ++j) { asm volatile("" : "+v"(gq[j]) :: "memory"); bad |= BAD(gq[j]); }
        if (!ANY(bad)) break;
        __builtin_amdgcn_s_sleep(CFG_GS1);
      }
      g = f32x2{asf(gq[0][0]), asf(gq[0][2])};
#pragma unroll
      for (int j = 1; j < NP / 2; ++j) g += f32x2{asf(gq[j][0]), asf(gq[j][2])};
      g = f32x2{half_sum(g[0]), half_sum(g[1])};
    }
    if (!ffn) {
      if (lane < 32) l_q[wv * 32 + lane] = pk2(g[0], g[1]);
      if (lane == 0) { l_q[64] = tlo; l_q[65] = thi; }
      asm volatile("s_waitcnt lgkmcnt(0)" ::: "memory");
      if (lane == 0) __hip_atomic_fetch_add(l_q + 66, 1u, __ATOMIC_RELAXED, __HIP_MEMORY_SCOPE_WORKGROUP);
      TR(1, 139);
      if (wv == 1) {
        const u32 t0 = (LDS_LD(l_q + 72) + CFG_ROFF) * 4 + TAU, tk = t0 + (2 * NT - 2) * TAU + CFG_FX + (NB - CFG_E0) * CFG_ES + CFG_VG;
        while ((int)(NOW() + 2000 - tk) < 0) __builtin_amdgcn_s_sleep(127);
        while ((int)(NOW() - tk) < 0 && LDS_LD(l_q + 67) == 0) __builtin_amdgcn_s_sleep(4);
        if (lane == 0) l_q[70] = tk + (1 << 30);
        asm volatile("s_waitcnt lgkmcnt(0)" ::: "memory");
        if (lane == 0) l_q[71] = 0;
      } else if (CFG_BAR) {
        _Pragma("unroll 1") for (int i = NB; i < 2 * NS; ++i) asm volatile("s_barrier\ns_sleep " STR(CFG_BS) ::: "memory");
      }
      u32 ytag = lane < 32 ? tlo : thi, yoff = WS_Y + (s * 4 + (hi & 3)) * 4096 + h * 128 + lo * 16 + (lane >> 5) * 8, ooff = WS_O + wg * 1024 + vo, ysrc = (lane >> 5) * 1024 + (lane & 31) * 8;
      asm volatile("" : "+v"(ytag), "+v"(yoff), "+v"(ooff), "+v"(ysrc));
      lds_barrier();
      if (wv == 0) {
        f32x2 ov[8];
        const f32x4 la = *(const f32x4*)(l_acc + 8 * 128), lb = *(const f32x4*)(l_acc + 8 * 128 + 4);
        const float Mg = l_acc[8 * 128 + 8];
#pragma unroll
        for (int w = 0; w < 8; ++w) ov[w] = *(const f32x2*)(l_acc + w * 128 + lane * 2);
        SB();
        const f32x2 l2 = (f32x2{la[0], la[1]} + f32x2{la[2], la[3]}) + (f32x2{lb[0], lb[1]} + f32x2{lb[2], lb[3]});
        const float Lg = l2[0] + l2[1], rl = __builtin_amdgcn_rcpf(Lg), wt = Mg + __builtin_amdgcn_logf(Lg);
        const f32x2 o = (((ov[0] + ov[1]) + (ov[2] + ov[3])) + ((ov[4] + ov[5]) + (ov[6] + ov[7]))) * f32x2{rl, rl};
        const u32x4 pc4 = {asu(o[0]), asu(o[1]), asu(wt), tlo};
        BST(pc4, rw, ooff, AUX_ST);
        *(u32x4*)(l_s + lane * 4) = pc4;
      }
      lds_barrier();
      lds_barrier();
      if (wv == 0) {
        const float* src = (const float*)((const char*)l_y + ysrc);
        const f32x2 t = (*(const f32x2*)src + *(const f32x2*)(src + 64)) + (*(const f32x2*)(src + 128) + *(const f32x2*)(src + 192));
        const auto z = __builtin_amdgcn_permlane32_swap(asu(t[0]), asu(t[1]), false, false);
        __builtin_amdgcn_raw_buffer_store_b64(u32x2{asu(asf(z[0]) + asf(z[1])), ytag}, rw, yoff, 0, AUX_ST);
      }
    } else {
      {
        float h0 = rbf(rbf(g[0]) + lo16(b1v)), h1 = rbf(rbf(g[1]) + hi16(b1v));
        h0 = h0 > 0.f ? h0 : 0.f; h1 = h1 > 0.f ? h1 : 0.f;
        if (lane < 32) *(f32x2*)(l_h + lane * 2) = f32x2{rbf(h0 * h0), rbf(h1 * h1)};
        asm volatile("" :: "v"(wu));
        asm volatile("s_waitcnt lgkmcnt(0)" ::: "memory");
        if (CFG_BAR) { _Pragma("unroll 1") for (int i = NB; i < 2 * NS; ++i) asm volatile("s_barrier\ns_sleep " STR(CFG_BS) ::: "memory"); }
        else { const u32 tk = tbase * 4 + (2 * NT - 4) * TAU; while ((int)(NOW() - tk) < 0) __builtin_amdgcn_s_sleep(127); }
        const u32 t2 = (LDS_LD(l_q + 72) + CFG_ROFF) * 4 + (2 * NT - 1) * TAU + (NB - CFG_E0) * CFG_ES + CFG_VG + CFG_W2;
        while (LDS_LD(l_q + 67) == 0 && (CFG_W2 == 9999 || (int)(NOW() - t2) < 0)) __builtin_amdgcn_s_sleep(4);
        asm volatile("" ::: "memory");
        const rsrc_t r2 = mkrsrc(w2, FF * DM * 2);
        u32x4 wb[7];
        if (CFG_WF) {
          const u32 tw = (LDS_LD(l_q + 72) + CFG_ROFF) * 4 + (2 * NT - 1) * TAU + (NB - CFG_E0) * CFG_ES + CFG_VG + CFG_WF;
          lds_barrier();
          while ((int)(NOW() - tw) < 0) __builtin_amdgcn_s_sleep(1);
        }
#pragma unroll
        for (int j = 0; j < 7; ++j) wb[j] = BLD(r2, hi * (DM * 2) + lo * 16, (h * FG + j * 8) * (DM * 2) + s * 128, AUX_W);
        asm volatile("" ::: "memory");
        if (!CFG_WF) lds_barrier();
        lds_barrier();
        f32x2 y[4] = {{0, 0}, {0, 0}, {0, 0}, {0, 0}};
#pragma unroll
        for (int j = 0; j < 7; ++j) {
          const float hh = l_h[j * 8 + hi];
#pragma unroll
          for (int k = 0; k < 4; ++k) y[k] = __builtin_elementwise_fma(f32x2{hh, hh}, f32x2{lo16(wb[j][k]), hi16(wb[j][k])}, y[k]);
        }
        const f32x2 t = hi8_fold(y);
        BST((u32x4{asu(t[0]), tlo, asu(t[1]), thi}), rw, (lane & 8) ? WS_BYTES : WS_B + (s * 4 + (lo >> 1)) * 4096 + h * 128 + (vop & 127), AUX_ST);
      }
      const bool fin = s == (((h >> 2) + 1 + (h & 3)) & 7);
      const int voy = vo, soy = h * 4096, YS = 1024;
      const u32 xb = lane < 32 ? xo : bo;
      float add = r & 1 ? hi16(xb) : lo16(xb);
      u32 ooff = lane & 40 ? 32 : lo * 4 + r * 2;
      asm volatile("" : "+v"(add), "+v"(ooff));
      lds_barrier();
      if (fin) {
        if (CFG_TQ) { const u32 tq = (LDS_LD(l_q + 72) + CFG_ROFF) * 4 + (2 * NT - 1) * TAU + (NB - CFG_E0) * CFG_ES + CFG_VG + CFG_TQ; while ((int)(NOW() - tq) < 0) __builtin_amdgcn_s_sleep(1); }
        u32x32 pz0, pz1, pz2;
        float a0, a1, c0, c1, pu;
        u32 pt;
#define LD2(i) PLD(8*i, v0, sa) PLD(8*i+1, v1, sa) PLD(8*i+2, v2, sa) PLD(8*i+3, v3, sa) PLD(8*i+4, v0, sb) PLD(8*i+5, v1, sb) PLD(8*i+6, v2, sb) PLD(8*i+7, v3, sb)
#define SUM2(d, n, k) "v_add_f32 %[" #d "], " PV(n, k) ", " PV(n+1, k) "\nv_add_f32 %[u], " PV(n+2, k) ", " PV(n+3, k) "\nv_add_f32 %[" #d "], %[" #d "], %[u]\n"
#define QW0 "12"
#define QW1 "8"
#define PA2 LD2(0) GAP2 LD2(1) ".Lpoll%=:\n" CK2(0) CK2(1)
#define CK2(i) "s_waitcnt vmcnt(" QW0 ")\n" PCK0(8*i) PCK(8*i+1) PCK(8*i+2) PCK(8*i+3) SUM2(c0, 8*i, 0) SUM2(c1, 8*i, 2) \
               "s_waitcnt vmcnt(" QW1 ")\n" PCK(8*i+4) PCK(8*i+5) PCK(8*i+6) PCK(8*i+7) SUM2(a0, 8*i+4, 0) SUM2(a1, 8*i+4, 2) PDROP LD2(i)
#define GAP2 "s_sleep " STR(CFG_PS2) "\n"
        asm volatile("s_nop 4\n" PA2 PEND
                     : "=&{v[40:71]}"(pz0), "=&{v[72:103]}"(pz1), "=&{v[104:135]}"(pz2), [a0] "=&v"(a0), [a1] "=&v"(a1), [c0] "=&v"(c0), [c1] "=&v"(c1), [u] "=&v"(pu), [t] "=&v"(pt)
                     : [v0] "v"(voy), [v1] "v"(voy + YS), [v2] "v"(voy + 2 * YS), [v3] "v"(voy + 3 * YS), [rs] "s"(rws), [sa] "s"(WS_B + soy), [sb] "s"(WS_Y + soy), [tl] "s"(tlo), [th] "s"(thi)
                     : "vcc", "scc", "memory");
        const auto z0 = __builtin_amdgcn_permlane32_swap(asu(a0), asu(c0), false, false), z1 = __builtin_amdgcn_permlane32_swap(asu(a1), asu(c1), false, false);
        const auto z2 = __builtin_amdgcn_permlane16_swap(asu(asf(z0[0]) + asf(z0[1])), asu(asf(z1[0]) + asf(z1[1])), false, false);
        float t = asf(z2[0]) + asf(z2[1]);
        t += dpp<XOR8>(t);
        t = half_sum(rbf(rbf(t) + add));
        __builtin_amdgcn_raw_buffer_store_b16((u16)pk2(t, t), mkrsrc(out + h * 16, 32), ooff, 0, AUX_ST);
        __builtin_amdgcn_raw_buffer_store_b32(lane & 1 ? thi : tlo, rw, h == 0 && lane < 32 ? WS_GEN + lane * 4 : WS_BYTES, 0, AUX_ST);
        asm volatile("s_waitcnt vmcnt(0)" :: "{v[40:71]}"(pz0), "{v[72:103]}"(pz1), "{v[104:135]}"(pz2));
      }
    }
    return;
  }

  u32 due = (LDS_LD(l_q + 72) + CFG_ROFF) * 4 + TAU;
  const u32 tw = due + (2 * NT - 2) * TAU + (NB - CFG_E0) * CFG_ES + CFG_VG + CFG_WT;
  u32 per = (due + TAU - 1 - ph0) / TAU;
  due = per * TAU + ph0;
  u32 ns = 0;
  u64 pend = (1ull << (NS - 1)) - 1;
  const u32 rtab = sb ^ row_off(lane);
  u32 row[NB];
  row[0] = NS - 1;
#define PACE() do { while ((int)(NOW() - due) < -CFG_PF0) __builtin_amdgcn_s_sleep(1); if (CFG_PF0) while ((int)(NOW() - due) < 0) { } due += TAU; } while (0)
#define WDELAY() do { if (CFG_WD) { if (wave & 1) __builtin_amdgcn_s_sleep(CFG_WD >> 8); if (wave & 2) __builtin_amdgcn_s_sleep(CFG_WD >> 4 & 15); if (wave & 4) __builtin_amdgcn_s_sleep(CFG_WD & 15); } } while (0)
#define ARRIVE(th_) do { const u64 gn_ = __hip_atomic_load((u64*)(l_q + 70), __ATOMIC_RELAXED, __HIP_MEMORY_SCOPE_WORKGROUP); const u32 now_ = NOW(); \
    const int off_ = __builtin_amdgcn_readfirstlane((u32)(gn_ >> 32)) * TAU, late_ = (int)(now_ - due) + (th_); \
    if (late_ > off_ && (int)(now_ - __builtin_amdgcn_readfirstlane((u32)gn_)) > 0) { if (lane == 0) __hip_atomic_fetch_max((int*)l_q + 71, late_ / TAU + 1, __ATOMIC_RELAXED, __HIP_MEMORY_SCOPE_WORKGROUP); asm volatile("" ::: "memory"); } } while (0)
#define PACEL() do { if (CFG_CS) while ((int)(NOW() - due - ns * TAU) < -CFG_CT) __builtin_amdgcn_s_sleep(CFG_CS); \
    while ((int)(NOW() - due - (ns = LDS_LD(l_q + 71)) * TAU) < -CFG_PF) __builtin_amdgcn_s_sleep(CFG_SL); \
    const u32 tg_ = due + ns * TAU; if (CFG_PF) while ((int)(NOW() - tg_) < 0) { if (CFG_PN) asm volatile("s_nop " STR(CFG_PN) "\ns_nop " STR(CFG_PN)); } due += TAU; WDELAY(); } while (0)
#define NEXT(sl, rs_) do { \
    const u32 w_ = per + ns; \
    row[sl] = (w_ - __builtin_clzll(pend << (~w_ & 63) | pend >> (w_ + 1 & 63))) & 63; \
    pend &= ~(1ull << row[sl]); ++per; \
    ISSUE(sl, rs_, __builtin_amdgcn_readlane(rtab, row[sl])); } while (0)
  if (CFG_SP == 1 && wave < 4) __builtin_amdgcn_s_setprio(3);
  if (CFG_SP == 2 && wave >= 4) __builtin_amdgcn_s_setprio(3);
  const rsrc_t rv = mkrsrc((const char*)V + (size_t)h * HEAD, HEAD);
  asm volatile("" ::: "memory");
  SB();
#pragma unroll
  for (int t = 1; t < NB; ++t) {
    PACE();
    if (t >= CFG_E0) due += CFG_ES;
    NEXT(t, rk);
    TR(0, t);
    SB();
  }
  while (__hip_atomic_load(l_q + 66, __ATOMIC_RELAXED, __HIP_MEMORY_SCOPE_WORKGROUP) != 2) __builtin_amdgcn_s_sleep(1);
  asm volatile("" ::: "memory");
  TR(1, 138);
  asm volatile("" :: "v"(pf));
  const u32x4 qp = *(const u32x4*)(l_q + c * 4);
  const u32 tlo = __builtin_amdgcn_readfirstlane(l_q[64]), thi = __builtin_amdgcn_readfirstlane(l_q[65]);
  const bool q0 = lane & 1, q1 = lane & 2;
  float* const ls = l_s + wave * (NT * 16) + r * 4 + (lane & 3);
  float m = -__builtin_inff(), l = 0.f, sc;
  f32x2 acc[4] = {{0, 0}, {0, 0}, {0, 0}, {0, 0}};
#define SCORE(sl) do { \
    float d[U]; \
    _Pragma("unroll") for (int u = 0; u < U; ++u) { \
      d[u] = dot2(tb[sl][u][0], qp[0], 0.f); d[u] = dot2(tb[sl][u][1], qp[1], d[u]); d[u] = dot2(tb[sl][u][2], qp[2], d[u]); d[u] = dot2(tb[sl][u][3], qp[3], d[u]); } \
    const float e0 = (q0 ? d[1] : d[0]) + dpp<XOR1>(q0 ? d[0] : d[1]), e1 = (q0 ? d[3] : d[2]) + dpp<XOR1>(q0 ? d[2] : d[3]); \
    float f = (q1 ? e1 : e0) + dpp<XOR2>(q1 ? e0 : e1); \
    f += dpp<XOR8>(f); f += dpp<ROR4>(f); \
    sc = rbf(rbf(f) * 0.08838834764831845f) * 1.4426950408889634f; \
  } while (0)
#define KTILE(sl) do { SCORE(sl); ls[row[sl] * 16] = sc; m = fmaxf(m, sc); asm volatile("" : "+v"(m) :: "memory"); } while (0)
#define ACCUM(sl) do { \
    const float p = __builtin_amdgcn_exp2f(sc - m); l += p; \
    const float pu[U] = {dpp<0x00>(p), dpp<0x55>(p), dpp<0xAA>(p), dpp<0xFF>(p)}; \
    _Pragma("unroll") for (int u = 0; u < U; ++u) \
      _Pragma("unroll") for (int k = 0; k < 4; ++k) acc[k] = __builtin_elementwise_fma(f32x2{pu[u], pu[u]}, f32x2{lo16(tb[sl][u][k]), hi16(tb[sl][u][k])}, acc[k]); \
    asm volatile("" : "+v"(acc[0]), "+v"(acc[1]), "+v"(acc[2]), "+v"(acc[3]), "+v"(l)); \
  } while (0)
#define VTILE(sl) do { sc = ls[row[sl] * 16]; ACCUM(sl); } while (0)
#define BUF(i) (((i) - NB) % D)
  constexpr int CH = (NB + D) / (D + 1), K1 = NB + (NS - NB - D) / D * D, V1 = D + (NS - D) / D * D;
#pragma unroll
  for (int k = 0; k <= D; ++k) {
    if (k < D) { TOUCH(k * CH + CH - 1); TR(1, k); if (!CFG_BAR) ARRIVE(CFG_T0); }
#pragma unroll
    for (int t = k * CH; t < k * CH + CH && t < NB; ++t) { if (CFG_BAR && k < D && t == k * CH + CH - 1) ARRIVE(0); KTILE(t); }
    SB();
    TR(1, 3 + k);
    if (k < D) { PACEL(); NEXT(k, rk); TR(0, NB + k); }
  }
  for (int i = NB + D; i < K1; i += D) {
#pragma unroll
    for (int ph = 0; ph < D; ++ph) {
      TOUCH(ph); ARRIVE(i < CFG_N0 ? CFG_T0 : CFG_TH); TR(1, i + ph - D);
      KTILE(ph);
      SB();
      PACEL();
      NEXT(ph, rk);
      TR(0, i + ph);
    }
  }
#pragma unroll
  for (int i = K1; i < NS + D; ++i) {
    TOUCH(BUF(i)); ARRIVE(CFG_TT); TR(1, i - D);
    KTILE(BUF(i));
    SB();
    PACEL();
    if (i == NS - 1) due += CFG_VG;
    if (i == NS) pend = ~0ull >> (64 - NS);
    if (i < NS) NEXT(BUF(i), rk); else NEXT(BUF(i), rv);
    TR(0, i);
  }
  m = fmaxf(m, dpp<XOR1>(m)); m = fmaxf(m, dpp<XOR2>(m)); m = xrow_max(m);
  if (lane == 0) {
    l_q[80 + wave] = asu(m);
    asm volatile("s_waitcnt lgkmcnt(0)" ::: "memory");
    __hip_atomic_fetch_add(l_q + 73, 1u, __ATOMIC_RELAXED, __HIP_MEMORY_SCOPE_WORKGROUP);
  }
  for (int i = D; i < V1; i += D) {
#pragma unroll
    for (int ph = 0; ph < D; ++ph) {
      TOUCH(ph); ARRIVE(i < CFG_TN ? CFG_TT : CFG_TH); TR(1, NS + i + ph - D);
      VTILE(ph);
      SB();
      PACEL();
      NEXT(ph, rv);
      TR(0, NS + i + ph);
    }
  }
  while (LDS_LD(l_q + 73) != NW) __builtin_amdgcn_s_sleep(1);
  asm volatile("" ::: "memory");
  const f32x4 ma = *(const f32x4*)(l_q + 80), mb = *(const f32x4*)(l_q + 84);
  const float mg = fmaxf(fmaxf(fmaxf(ma[0], ma[1]), fmaxf(ma[2], ma[3])), fmaxf(fmaxf(mb[0], mb[1]), fmaxf(mb[2], mb[3])));
  TOUCH(0); TR(1, 2 * NS - 2);
  VTILE(0);
  float pl[U];
  {
    const float f = __builtin_amdgcn_exp2f(m - mg), p = __builtin_amdgcn_exp2f(ls[row[1] * 16] - mg);
#pragma unroll
    for (int k = 0; k < 4; ++k) acc[k] *= f32x2{f, f};
    l = fmaf(l, f, p);
    l += dpp<XOR1>(l); l += dpp<XOR2>(l); l = xrow_sum(l);
    if (lane == 0) { l_acc[8 * 128 + wave] = l; l_acc[8 * 128 + 8] = mg; }
    pl[0] = dpp<0x00>(p); pl[1] = dpp<0x55>(p); pl[2] = dpp<0xAA>(p); pl[3] = dpp<0xFF>(p);
    asm volatile("" : "+v"(acc[0]), "+v"(acc[1]), "+v"(acc[2]), "+v"(acc[3]), "+v"(pl[0]), "+v"(pl[1]), "+v"(pl[2]), "+v"(pl[3]) :: "memory");
  }
  u32x4 ro = {(u32)(uintptr_t)wo, (u32)((uintptr_t)wo >> 32) & 0xffffu, NH * DH * DM * 2, 0x00020000};
  u32x4 woa, wob;
#define WOLD() asm volatile("buffer_load_dwordx4 %0, %2, %3, %4 offen nt\nbuffer_load_dwordx4 %1, %2, %3, %4 offen offset:1024 nt" : "=&{v[72:75]}"(woa), "=&{v[76:79]}"(wob) \
    : "v"(wov), "s"(ro), "s"(wos) : "memory")
  u32 wov = hi * 2 * (DM * 2) + lo * 16, wos = (h * DH + wave * 16) * (DM * 2) + s * 128, ia = wave * 128 + c * 8 + r * 2, iy = wave * 64 + lo * 8 + r * 2, im = (wave * 8 + hi) * 4;
  u32 pva = lo * 1024 + hi * 16, psa = WS_O + h * 8192 + wave * 128;
  u64 pex = ~(0x0101010101010101ull << s);
  asm volatile("" : "+v"(wov), "+s"(wos), "+s"(ro), "+v"(ia), "+v"(iy), "+v"(im), "+v"(pva), "+s"(psa), "+s"(pex));
  TR(0, 131);
  SB();
#pragma unroll
  for (int u = 0; u < U; ++u) {
#pragma unroll
    for (int k = 0; k < 4; ++k) acc[k] = __builtin_elementwise_fma(f32x2{pl[u], pl[u]}, f32x2{lo16(tb[1][u][k]), hi16(tb[1][u][k])}, acc[k]);
    SB();
  }
  *(f32x2*)(l_acc + ia) = xrow_fold(acc);
  WOLD();
  if (lane == 0) l_q[67] = 1;
  TR(1, 131);
  lds_barrier();
  TR(0, 132);
  u32x4 pz;
  {
    while ((int)(NOW() - tw) < CFG_TP) __builtin_amdgcn_s_sleep(1);
    f32x2 wa[4], wb[4];
#define UNP(d, s_) "v_lshlrev_b32 v[" #d "], 16, v[" #s_ "]\nv_and_b32 v[" #d "+1], 0xffff0000, v[" #s_ "]\n"
#define ADD3(ctl) "v_add_f32_dpp %[e], %[e], %[e] " ctl "v_add_f32_dpp %[o0], %[o0], %[o0] " ctl "v_add_f32_dpp %[o1], %[o1], %[o1] " ctl
#define D1 "quad_perm:[1,0,3,2] row_mask:0xf bank_mask:0xf\n"
#define D2 "quad_perm:[2,3,0,1] row_mask:0xf bank_mask:0xf\n"
#define D7 "row_half_mirror row_mask:0xf bank_mask:0xf\n"
    asm volatile("s_mov_b64 exec, %[ex]\n" PLD(0, va, sa) "s_mov_b64 exec, -1\ns_waitcnt lgkmcnt(0)\ns_barrier" : "=&{v[40:43]}"(pz) : [va] "v"(pva), [rs] "s"(rws), [sa] "s"(psa), [ex] "s"(pex) : "memory");
    const f32x4 mine = *(const f32x4*)(l_s + im);
    float M = mine[2], oa = mine[0], ob = mine[1], e;
    asm volatile("s_waitcnt vmcnt(1)\n" UNP(44, 72) UNP(46, 73) UNP(48, 74) UNP(50, 75) UNP(52, 76) UNP(54, 77) UNP(56, 78) UNP(58, 79)
                 "s_mov_b64 exec, %[ex]\n.Lpoll%=:\ns_waitcnt vmcnt(0)\nv_cmp_eq_u32 vcc, %[tl], v43\nv_mov_b32 %[m], v42\nv_mov_b32 %[o0], v40\nv_mov_b32 %[o1], v41\n"
                 "s_andn2_b64 exec, exec, vcc\ns_cbranch_execz .Lgot%=\n" PLD(0, va, sa) "s_branch .Lpoll%=\n.Lgot%=:\ns_mov_b64 exec, -1\ns_nop 1\n"
                 "v_max_f32_dpp %[e], %[m], %[m] " D1 "s_nop 1\nv_max_f32_dpp %[e], %[e], %[e] " D2 "s_nop 1\nv_max_f32_dpp %[e], %[e], %[e] " D7
                 "v_sub_f32 %[e], %[m], %[e]\nv_exp_f32 %[e], %[e]\ns_nop 0\nv_mul_f32 %[o0], %[e], %[o0]\nv_mul_f32 %[o1], %[e], %[o1]\ns_nop 0\n"
                 ADD3(D1) ADD3(D2) ADD3(D7) "v_rcp_f32 %[e], %[e]\ns_nop 0"
                 : "+{v[40:43]}"(pz), [o0] "+v"(oa), [o1] "+v"(ob), [m] "+v"(M), [e] "=&v"(e), "+{v[72:75]}"(woa), "+{v[76:79]}"(wob),
                   "=&{v[44:45]}"(wa[0]), "=&{v[46:47]}"(wa[1]), "=&{v[48:49]}"(wa[2]), "=&{v[50:51]}"(wa[3]), "=&{v[52:53]}"(wb[0]), "=&{v[54:55]}"(wb[1]), "=&{v[56:57]}"(wb[2]), "=&{v[58:59]}"(wb[3])
                 : [va] "v"(pva), [rs] "s"(rws), [sa] "s"(psa), [tl] "s"(tlo), [ex] "s"(pex)
                 : "vcc", "scc", "memory");
    TR(1, 132);
    f32x2 y[4];
#pragma unroll
    for (int k = 0; k < 4; ++k) y[k] = __builtin_elementwise_fma(f32x2{ob, ob}, wb[k], f32x2{oa, oa} * wa[k]);
    *(f32x2*)(l_y + iy) = hi8_fold(y) * f32x2{e, e};
  }
  lds_barrier();
  TR(0, 133);
  asm volatile(".Lcode_to:");
}

extern "C" void kernel_launch(void* const* d_in, const int* in_sizes, int n_in, void* d_out, int out_size, void* d_ws,
                              size_t ws_size, hipStream_t stream) {
  fused_kernel<<<NWG, NTHR, 0, stream>>>((const u16*)d_in[0], (const u16*)d_in[1], (const u16*)d_in[2], (const u16*)d_in[3],
                                         (const u16*)d_in[4], (const u16*)d_in[6], (const u16*)d_in[7], (const u16*)d_in[8],
                                         (const u16*)d_in[9], (u16*)d_out, (unsigned char*)d_ws);
}
```
